```python
import torch

def _softmax(x, axis=-1):
    return torch.softmax(x, dim=axis)

def _gelu(x):
    return torch.nn.functional.gelu(x, approximate='tanh')

def _silu(x):
    return torch.nn.functional.silu(x)

def _sigmoid(x):
    return torch.sigmoid(x)

def _layernorm(x, axis=-1, eps=1e-05):
    m = x.mean(dim=axis, keepdim=True)
    v = x.var(dim=axis, keepdim=True, unbiased=False)
    return (x - m) / torch.sqrt(v + eps)

def _scatter_add(tgt, idx, src, axis=0):
    return tgt.clone().index_add_(axis, idx, src)

def _segment_sum(src, seg, n):
    out = torch.zeros((n,) + src.shape[1:], dtype=src.dtype)
    return out.index_add_(0, seg, src)

def _pad(x, pw):
    flat = tuple((v for pair in reversed(pw) for v in pair))
    return torch.nn.functional.pad(x, flat)
kernel_name = 'adaLN_cond__spectral_gfnet__glu_gated_reglu__prompt'

def setup_inputs():

    _w = lambda a: (lambda t: t.to(torch.bfloat16) if t.is_floating_point() else t)(torch.as_tensor(a))

    def _A_setup():
        B, S, D = (1, 2048, 640)
        Df = D // 2 + 1
        return {'x': _w((lambda *s: torch.randn(*s, dtype=torch.bfloat16) * 0.05)(B, S, D)), 'w_r': _w((lambda *s: torch.randn(*s, dtype=torch.bfloat16) * 1.6)(S, Df) * 0.02), 'w_i': _w((lambda *s: torch.randn(*s, dtype=torch.bfloat16) * 1.6)(S, Df) * 0.02)}
    _A_in = _A_setup()

    def _B_setup():
        B, S, D, F = (1, 2048, 640, 1536)
        return {'x': _w((lambda *s: torch.randn(*s, dtype=torch.bfloat16) * 0.05)(B, S, D)), 'W_gate': _w((lambda *s: torch.randn(*s, dtype=torch.bfloat16) * 0.4)(D, F) * (2.0 / D) ** 0.5), 'W_up': _w((lambda *s: torch.randn(*s, dtype=torch.bfloat16) * 0.4)(D, F) * (2.0 / D) ** 0.5), 'W_down': _w((lambda *s: torch.randn(*s, dtype=torch.bfloat16) * 0.4)(F, D) * (1.0 / F) ** 0.5)}
    _B_in = _B_setup()
    return {'A_x': _A_in['x'], 'A_w_r': _A_in['w_r'], 'A_w_i': _A_in['w_i'], 'B_x': _B_in['x'], 'B_W_gate': _B_in['W_gate'], 'B_W_up': _B_in['W_up'], 'B_W_down': _B_in['W_down'], 'c': _w((lambda *s: torch.randn(*s, dtype=torch.bfloat16) * 0.2)(1, 640) * 8.0), 'w_mod': _w((lambda *s: torch.randn(*s, dtype=torch.bfloat16) * 0.2)(640, 6 * 640) * 640 ** (-0.5) * 8.0)}

def reference(A_x, A_w_r, A_w_i, B_x, B_W_gate, B_W_up, B_W_down, c, w_mod):

    def _A_ref(x, w_r, w_i):
        B, S, D = x.shape
        X = (lambda x, axes=(-2, -1): torch.fft.rfft2(x.float(), dim=axes))(x, axes=(-2, -1))
        W = w_r + 1j * w_i
        X = X * W
        y = (lambda x, s, axes=(-2, -1): torch.fft.irfft2(x, s=s, dim=axes).to(torch.bfloat16))(X, (S, D), axes=(-2, -1))
        return y

    def _B_ref(x, W_gate, W_up, W_down):
        gate = torch.einsum('bsd,df->bsf', x, W_gate)
        up = torch.einsum('bsd,df->bsf', x, W_up)
        gate = gate * (lambda x, dt: x.to(dt))(gate > 0, gate.dtype)
        h = gate * up
        return torch.einsum('bsf,fd->bsd', h, W_down)
    x = A_x
    mod = (_silu(c) @ w_mod).reshape(c.shape[0], 6, 1, -1)
    sa, ca, ga, sb, cb, gb = (mod[:, 0].clone(), mod[:, 1].clone(), mod[:, 2].clone(), mod[:, 3].clone(), mod[:, 4].clone(), mod[:, 5].clone())
    h = _layernorm(x, axis=-1) * (1 + ca) + sa
    x = x + ga * _A_ref(h, A_w_r, A_w_i)
    h = _layernorm(x, axis=-1) * (1 + cb) + sb
    x = x + gb * _B_ref(h, B_W_gate, B_W_up, B_W_down)
    return x
```

```cpp
#include <hip/hip_runtime.h>
#include <hip/hip_bf16.h>
#include <utility>

typedef unsigned u32;
typedef unsigned u32x2 __attribute__((ext_vector_type(2)));
typedef unsigned u32x4 __attribute__((ext_vector_type(4)));
typedef float f32x2 __attribute__((ext_vector_type(2)));
typedef float f32x4 __attribute__((ext_vector_type(4)));
typedef __bf16 bf16x2 __attribute__((ext_vector_type(2)));
typedef __amdgpu_buffer_rsrc_t rsrc_t;
#define DI __device__ __forceinline__
#define LAS __attribute__((address_space(3)))
#define SC1 16

constexpr u32 OFF_EPOCH = 0, OFF_TMO = 256, OFF_MODG = 0x1000, OFF_PREPF = 0x5000, OFF_H2F = 0x9000;
constexpr u32 E1P = 11264, E1R = 1408, E2P = 12288;
constexpr u32 OFF_E1 = 0x10000, OFF_E2 = 0x2D0000, OFF_H2 = 0x5D0000, OFF_X1 = 0x850000, OFF_WG1 = 0xAD0000,
              OFF_WG2 = 0xE90000, OFF_H = 0x1070000, WS_USED = 0x1670000, OFF_STAMP = 0x1F00000;
constexpr u32 OOB = 0x7ff00000u;
constexpr u32 L_XS = 0, L_RED = 10240, L_MSC = 10752, L_M3 = 16512, L_S3 = 26752, S3P = 2688, L_S3D = L_S3 + 4 * S3P,
              L_KI = 39040, KIP = 1328, L_WL = 53648, L_KO = 68256, L_S3B = 78496, L_S3BD = L_S3B + 4 * S3P,
              L_XT = 90784, L_P1 = 101024, P1P = 400, L_P2 = 117024, P2P = 176, LDS_BYTES = 163840;
constexpr u32 SPIN_CAP = 40000;

DI u32 cvtpk(float lo, float hi) { f32x2 v = {lo, hi}; bf16x2 b = __builtin_convertvector(v, bf16x2); return __builtin_bit_cast(u32, b); }
DI float bflo(u32 u) { return __builtin_bit_cast(float, u << 16); }
DI float bfhi(u32 u) { return __builtin_bit_cast(float, u & 0xffff0000u); }
DI float rbf(float x) { return bflo(cvtpk(x, x)); }
DI u32 nosent(u32 v) { return v < 0xfffffffeu ? v : 0xfffffffeu; }
DI float cosr(float rev) { return __builtin_amdgcn_cosf(rev); }
DI float sinr(float rev) { return __builtin_amdgcn_sinf(rev); }
DI u32 max3u(u32 a, u32 b, u32 c) { u32 m = a > b ? a : b; return m > c ? m : c; }

DI u32x4 ldg4(rsrc_t r, u32 off, int aux) { return aux ? __builtin_amdgcn_raw_buffer_load_b128(r, off, 0, SC1) : __builtin_amdgcn_raw_buffer_load_b128(r, off, 0, 0); }
DI u32x2 ldg2(rsrc_t r, u32 off, int aux) { return aux ? __builtin_amdgcn_raw_buffer_load_b64(r, off, 0, SC1) : __builtin_amdgcn_raw_buffer_load_b64(r, off, 0, 0); }
DI u32 ldg1(rsrc_t r, u32 off, int aux) { return aux ? __builtin_amdgcn_raw_buffer_load_b32(r, off, 0, SC1) : __builtin_amdgcn_raw_buffer_load_b32(r, off, 0, 0); }
DI void stg4(rsrc_t r, u32 off, u32x4 v) { __builtin_amdgcn_raw_buffer_store_b128(v, r, off, 0, SC1); }
DI void stg2(rsrc_t r, u32 off, u32x2 v) { __builtin_amdgcn_raw_buffer_store_b64(v, r, off, 0, SC1); }
DI void stg1(rsrc_t r, u32 off, u32 v) { __builtin_amdgcn_raw_buffer_store_b32(v, r, off, 0, SC1); }
template <class T> DI T lds_ld(u32 a) { return *(const LAS T*)(size_t)a; }
template <class T> DI void lds_st(u32 a, T v) { *(LAS T*)(size_t)a = v; }
#define WAIT_VM(n) asm volatile("s_waitcnt vmcnt(" #n ")" ::: "memory")
DI void bar() { asm volatile("s_waitcnt lgkmcnt(0)\n\ts_barrier" ::: "memory"); }

DI void swap32u(u32& a, u32& b) { auto r = __builtin_amdgcn_permlane32_swap(a, b, false, false); a = r[0]; b = r[1]; }
DI void swap16u(u32& a, u32& b) { auto r = __builtin_amdgcn_permlane16_swap(a, b, false, false); a = r[0]; b = r[1]; }
DI void swap32(float& a, float& b) { u32 x = __builtin_bit_cast(u32, a), y = __builtin_bit_cast(u32, b); swap32u(x, y); a = __builtin_bit_cast(float, x); b = __builtin_bit_cast(float, y); }
DI void swap16(float& a, float& b) { u32 x = __builtin_bit_cast(u32, a), y = __builtin_bit_cast(u32, b); swap16u(x, y); a = __builtin_bit_cast(float, x); b = __builtin_bit_cast(float, y); }
template <int N> DI float ror_add(float v) {
  return v + __builtin_bit_cast(float, __builtin_amdgcn_mov_dpp(__builtin_bit_cast(int, v), 0x120 + N, 0xf, 0xf, true));
}
DI float row_sum(float v) { v = ror_add<8>(v); v = ror_add<4>(v); v = ror_add<2>(v); return ror_add<1>(v); }
DI void wave_sum2(float a, float b, float& sa, float& sb) {
  swap32(a, b); float v = a + b, u = v; swap16(v, u); v = row_sum(v + u);
  sa = __builtin_bit_cast(float, __builtin_amdgcn_readlane(__builtin_bit_cast(int, v), 0));
  sb = __builtin_bit_cast(float, __builtin_amdgcn_readlane(__builtin_bit_cast(int, v), 32));
}

#define M32 "v_mfma_f32_16x16x32_bf16 "
#define M16 "v_mfma_f32_16x16x16_bf16 "
template <bool DA> DI void mm3(f32x4 (&re)[3], f32x4 (&im)[3], const u32x4 (&d)[3], u32x4 fr, u32x4 fi) {
  if (DA)
    asm volatile("s_nop 1\n\t" M32 "%0, %6, %9, 0\n\t" M32 "%3, %6, %10, 0\n\t" M32 "%1, %7, %9, 0\n\t" M32 "%4, %7, %10, 0\n\t" M32 "%2, %8, %9, 0\n\t" M32 "%5, %8, %10, 0\n\ts_nop 7"
                 : "=&v"(re[0]), "=&v"(re[1]), "=&v"(re[2]), "=&v"(im[0]), "=&v"(im[1]), "=&v"(im[2]) : "v"(d[0]), "v"(d[1]), "v"(d[2]), "v"(fr), "v"(fi));
  else
    asm volatile("s_nop 1\n\t" M32 "%0, %9, %6, 0\n\t" M32 "%3, %10, %6, 0\n\t" M32 "%1, %9, %7, 0\n\t" M32 "%4, %10, %7, 0\n\t" M32 "%2, %9, %8, 0\n\t" M32 "%5, %10, %8, 0\n\ts_nop 7"
                 : "=&v"(re[0]), "=&v"(re[1]), "=&v"(re[2]), "=&v"(im[0]), "=&v"(im[1]), "=&v"(im[2]) : "v"(d[0]), "v"(d[1]), "v"(d[2]), "v"(fr), "v"(fi));
}
DI void mm6k16(f32x4 (&o)[6], u32x2 m, const u32x2 (&d)[6]) {
  asm volatile("s_nop 1\n\t" M16 "%0, %6, %7, 0\n\t" M16 "%1, %6, %8, 0\n\t" M16 "%2, %6, %9, 0\n\t" M16 "%3, %6, %10, 0\n\t" M16 "%4, %6, %11, 0\n\t" M16 "%5, %6, %12, 0\n\ts_nop 7"
               : "=&v"(o[0]), "=&v"(o[1]), "=&v"(o[2]), "=&v"(o[3]), "=&v"(o[4]), "=&v"(o[5])
               : "v"(m), "v"(d[0]), "v"(d[1]), "v"(d[2]), "v"(d[3]), "v"(d[4]), "v"(d[5]));
}
template <int SG> DI u32x4 cmul_pack(f32x4 re, f32x4 im, f32x4 c, f32x4 s) {
  f32x4 a, b;
  if (SG > 0) { a = re * c - im * s; b = im * c + re * s; } else { a = re * c + im * s; b = im * c - re * s; }
  return u32x4{cvtpk(a[0], b[0]), cvtpk(a[1], b[1]), cvtpk(a[2], b[2]), cvtpk(a[3], b[3])};
}

#define ACLOB "a0","a1","a2","a3","a4","a5","a6","a7","a8","a9","a10","a11","a12","a13","a14","a15","a16","a17","a18","a19","a20","a21","a22","a23","a24","a25","a26","a27","a28","a29","a30","a31","a32","a33","a34","a35","a36","a37","a38","a39","a40","a41","a42","a43","a44","a45","a46","a47","a48","a49","a50","a51","a52","a53","a54","a55","a56","a57","a58","a59","a60","a61","a62","a63","a64","a65","a66","a67","a68","a69","a70","a71","a72","a73","a74","a75","a76","a77","a78","a79","a80","a81","a82","a83","a84","a85","a86","a87","a88","a89","a90","a91","a92","a93","a94","a95","a96","a97","a98","a99","a100","a101","a102","a103","a104","a105","a106","a107","a108","a109","a110","a111","a112","a113","a114","a115","a116","a117","a118","a119","a120","a121","a122","a123","a124","a125","a126","a127","a128","a129","a130","a131","a132","a133","a134","a135","a136","a137","a138","a139","a140","a141","a142","a143","a144","a145","a146","a147","a148","a149","a150","a151","a152","a153","a154","a155","a156","a157","a158","a159","a160","a161","a162","a163","a164","a165","a166","a167","a168","a169","a170","a171","a172","a173","a174","a175","a176","a177","a178","a179","a180","a181","a182","a183","a184","a185","a186","a187","a188","a189","a190","a191","a192","a193","a194","a195","a196","a197","a198","a199","a200","a201","a202","a203","a204","a205","a206","a207","a208","a209","a210","a211","a212","a213","a214","a215","a216","a217","a218","a219","a220","a221","a222","a223","a224","a225","a226","a227","a228","a229","a230","a231","a232","a233","a234","a235","a236","a237","a238","a239"
template <int Q, int IMM> DI void load_a(u32 voff, rsrc_t rs, u32 soff) {
  asm volatile("buffer_load_dwordx4 a[%c3:%c4], %0, %1, %2 offen offset:%c5 sc1" ::"v"(voff), "s"(rs), "s"(soff), "i"(4 * Q), "i"(4 * Q + 3), "i"(IMM) : "memory", ACLOB);
}
template <int C> DI void load_w1_slot(u32 voff, rsrc_t rs, u32 base, u32 rot) {
  u32 cc = C + rot; cc = cc >= 10 ? cc - 10 : cc;
  const u32 s0 = base + cc * 6144, s1 = s0 + 4096;
  load_a<6 * C + 0, 0>(voff, rs, s0); load_a<6 * C + 1, 1024>(voff, rs, s0); load_a<6 * C + 2, 2048>(voff, rs, s0);
  load_a<6 * C + 3, 3072>(voff, rs, s0); load_a<6 * C + 4, 0>(voff, rs, s1); load_a<6 * C + 5, 1024>(voff, rs, s1);
}
template <int F> DI void load_w2(u32 voff, rsrc_t rs, u32 base, u32 rot2) {
  const u32 pp = (F / 15 + rot2) & 3;
  load_a<F, (F % 15 % 4) * 1024>(voff, rs, base + pp * 15360 + (F % 15 / 4) * 4096);
}
DI void dma16(u32 voff, rsrc_t rs, u32 soff, u32 ldsa) {
  asm volatile("s_mov_b32 m0, %3\n\ts_nop 0\n\tbuffer_load_dwordx4 %0, %1, %2 offen sc1 lds" ::"v"(voff), "s"(rs), "s"(soff), "s"(ldsa) : "memory");
}

struct G1 { f32x4 acc[8][3]; u32x4 fr[8]; u32 ad[2][3]; };
template <int KS, int T> DI void g1_group(G1& s) {
  constexpr int q = 12 * KS, N = KS + 1, C = N >> 1;
  if constexpr (KS == 0) {
    asm volatile("s_waitcnt lgkmcnt(7)\n\t" M32 "%0, a[%c5:%c6], %3, 0\n\t" M32 "%1, a[%c7:%c8], %3, 0\n\t" M32 "%2, a[%c9:%c10], %3, 0\n\tds_read_b128 %3, %4 offset:%c11"
                 : "=&v"(s.acc[T][0]), "=&v"(s.acc[T][1]), "=&v"(s.acc[T][2]), "+v"(s.fr[T])
                 : "v"(s.ad[N & 1][C >> 2]), "i"(q), "i"(q + 3), "i"(q + 4), "i"(q + 7), "i"(q + 8), "i"(q + 11), "i"((C & 3) * 16384 + T * 2048) : "memory");
  } else if constexpr (KS < 19) {
    asm volatile("s_waitcnt lgkmcnt(7)\n\t" M32 "%0, a[%c5:%c6], %3, %0\n\t" M32 "%1, a[%c7:%c8], %3, %1\n\t" M32 "%2, a[%c9:%c10], %3, %2\n\tds_read_b128 %3, %4 offset:%c11"
                 : "+v"(s.acc[T][0]), "+v"(s.acc[T][1]), "+v"(s.acc[T][2]), "+v"(s.fr[T])
                 : "v"(s.ad[N & 1][C >> 2]), "i"(q), "i"(q + 3), "i"(q + 4), "i"(q + 7), "i"(q + 8), "i"(q + 11), "i"((C & 3) * 16384 + T * 2048) : "memory");
  } else {
    asm volatile("s_waitcnt lgkmcnt(%c10)\n\t" M32 "%0, a[%c4:%c5], %3, %0\n\t" M32 "%1, a[%c6:%c7], %3, %1\n\t" M32 "%2, a[%c8:%c9], %3, %2"
                 : "+v"(s.acc[T][0]), "+v"(s.acc[T][1]), "+v"(s.acc[T][2])
                 : "v"(s.fr[T]), "i"(q), "i"(q + 3), "i"(q + 4), "i"(q + 7), "i"(q + 8), "i"(q + 11), "i"(7 - T) : "memory");
  }
}
template <int KS> DI void g1_kstep(G1& s, u32 wv, rsrc_t rs, u32 w2base, u32 rot2) {
  if constexpr ((KS & 1) && KS < 19) {
    constexpr int C = (KS + 1) / 2, NV = 4 * (9 - C) + 3 * (KS >= 2 ? KS - 2 : 0);
    asm volatile("s_waitcnt vmcnt(%c0)\n\ts_barrier" ::"i"(NV) : "memory");
  }
  g1_group<KS, 0>(s); g1_group<KS, 1>(s); g1_group<KS, 2>(s);
  if constexpr (KS >= 2) load_w2<3 * (KS - 2) + 0>(wv, rs, w2base, rot2);
  g1_group<KS, 3>(s); g1_group<KS, 4>(s);
  if constexpr (KS >= 2) load_w2<3 * (KS - 2) + 1>(wv, rs, w2base, rot2);
  g1_group<KS, 5>(s); g1_group<KS, 6>(s);
  if constexpr (KS >= 2) load_w2<3 * (KS - 2) + 2>(wv, rs, w2base, rot2);
  g1_group<KS, 7>(s);
}
template <int... K> DI void g1_all(G1& s, u32 wv, rsrc_t rs, u32 w2base, u32 rot2, std::integer_sequence<int, K...>) { (g1_kstep<K>(s, wv, rs, w2base, rot2), ...); }

template <int P, int KK, int TT> DI void g2_group(f32x4 (&acc)[4][5], u32x4 h) {
  constexpr int q = 20 * (3 * P + KK);
  if constexpr (P == 0 && KK == 0)
    asm volatile(M32 "%0, a[%c6:%c7], %5, 0\n\t" M32 "%1, a[%c8:%c9], %5, 0\n\t" M32 "%2, a[%c10:%c11], %5, 0\n\t" M32 "%3, a[%c12:%c13], %5, 0\n\t" M32 "%4, a[%c14:%c15], %5, 0"
                 : "=&v"(acc[TT][0]), "=&v"(acc[TT][1]), "=&v"(acc[TT][2]), "=&v"(acc[TT][3]), "=&v"(acc[TT][4])
                 : "v"(h), "i"(q), "i"(q + 3), "i"(q + 4), "i"(q + 7), "i"(q + 8), "i"(q + 11), "i"(q + 12), "i"(q + 15), "i"(q + 16), "i"(q + 19));
  else
    asm volatile(M32 "%0, a[%c6:%c7], %5, %0\n\t" M32 "%1, a[%c8:%c9], %5, %1\n\t" M32 "%2, a[%c10:%c11], %5, %2\n\t" M32 "%3, a[%c12:%c13], %5, %3\n\t" M32 "%4, a[%c14:%c15], %5, %4"
                 : "+v"(acc[TT][0]), "+v"(acc[TT][1]), "+v"(acc[TT][2]), "+v"(acc[TT][3]), "+v"(acc[TT][4])
                 : "v"(h), "i"(q), "i"(q + 3), "i"(q + 4), "i"(q + 7), "i"(q + 8), "i"(q + 11), "i"(q + 12), "i"(q + 15), "i"(q + 16), "i"(q + 19));
}
#define HL(i, v, o) "buffer_load_dwordx4 %" #i ", %" #v ", %16, %17 offen offset:" #o " sc1\n\t"
#define HLALL HL(0, 12, 0) HL(1, 13, 0) HL(2, 14, 0) HL(3, 15, 0) HL(4, 12, 1024) HL(5, 13, 1024) HL(6, 14, 1024) HL(7, 15, 1024) HL(8, 12, 2048) HL(9, 13, 2048) HL(10, 14, 2048) HL(11, 15, 2048)
#define H12(c, h) c(h[0]), c(h[1]), c(h[2]), c(h[3]), c(h[4]), c(h[5]), c(h[6]), c(h[7]), c(h[8]), c(h[9]), c(h[10]), c(h[11])
template <bool FIRST> DI void h_load(u32x4 (&h)[12], rsrc_t rs, const u32 (&vo)[4], u32 soff) {
  if (FIRST) asm volatile(HLALL : H12("=&v", h) : "v"(vo[0]), "v"(vo[1]), "v"(vo[2]), "v"(vo[3]), "s"(rs), "s"(soff) : "memory");
  else asm volatile(HLALL : H12("+v", h) : "v"(vo[0]), "v"(vo[1]), "v"(vo[2]), "v"(vo[3]), "s"(rs), "s"(soff) : "memory");
}
DI bool h_ok(const u32x4 (&h)[12]) {
  u32 m = 0;
#pragma unroll
  for (int i = 0; i < 12; ++i) { m = max3u(m, h[i][0], h[i][1]); m = max3u(m, h[i][2], h[i][3]); }
  return __all(m != 0xffffffffu);
}
template <int P> DI void g2_prod(f32x4 (&acc)[4][5], u32x4 (&h)[12], rsrc_t rs, const u32 (&vo)[4], u32 soff, u32& fail) {
  if (P < 3) asm volatile("s_waitcnt vmcnt(12)" : H12("+v", h) :: "memory"); else asm volatile("s_waitcnt vmcnt(0)" : H12("+v", h) :: "memory");
  for (u32 sp = 0; !h_ok(h); ++sp) {
    if (sp > SPIN_CAP || fail) { fail = 1; break; }
    h_load<false>(h, rs, vo, soff);
    asm volatile("s_waitcnt vmcnt(0)" : H12("+v", h) :: "memory");
  }
  g2_group<P, 0, 0>(acc, h[0]); g2_group<P, 0, 1>(acc, h[1]); g2_group<P, 0, 2>(acc, h[2]); g2_group<P, 0, 3>(acc, h[3]);
  g2_group<P, 1, 0>(acc, h[4]); g2_group<P, 1, 1>(acc, h[5]); g2_group<P, 1, 2>(acc, h[6]); g2_group<P, 1, 3>(acc, h[7]);
  g2_group<P, 2, 0>(acc, h[8]); g2_group<P, 2, 1>(acc, h[9]); g2_group<P, 2, 2>(acc, h[10]); g2_group<P, 2, 3>(acc, h[11]);
}

DI u32x2 tr_read(u32 a) { u32x2 r; asm volatile("ds_read_b64_tr_b16 %0, %1" : "=v"(r) : "v"(a) : "memory"); return r; }

DI f32x2 strip_reduce(const u32x4 (&wv)[3], const float (&sc)[3]) {
  float a[8];
#pragma unroll
  for (int j = 0; j < 4; ++j) {
    a[2 * j] = sc[0] * bflo(wv[0][j]) + sc[1] * bflo(wv[1][j]) + sc[2] * bflo(wv[2][j]);
    a[2 * j + 1] = sc[0] * bfhi(wv[0][j]) + sc[1] * bfhi(wv[1][j]) + sc[2] * bfhi(wv[2][j]);
  }
  float s[4];
#pragma unroll
  for (int j = 0; j < 4; ++j) { swap32(a[j], a[j + 4]); s[j] = a[j] + a[j + 4]; }
  swap16(s[0], s[2]); swap16(s[1], s[3]);
  return f32x2{row_sum(s[0] + s[2]), row_sum(s[1] + s[3])};
}
DI void ln_stats(const float (&v)[2][10], float (&rs)[2], float (&ms)[2]) {
  float s0 = 0, s1 = 0;
#pragma unroll
  for (int i = 0; i < 10; ++i) { s0 += v[0][i]; s1 += v[1][i]; }
  float m0, m1; wave_sum2(s0, s1, m0, m1); m0 *= (1.f / 640); m1 *= (1.f / 640);
  float q0 = 0, q1 = 0;
#pragma unroll
  for (int i = 0; i < 10; ++i) { float a = v[0][i] - m0, b = v[1][i] - m1; q0 += a * a; q1 += b * b; }
  float t0, t1; wave_sum2(q0, q1, t0, t1);
  rs[0] = __builtin_amdgcn_rcpf(rbf(__builtin_amdgcn_sqrtf(rbf(rbf(t0 * (1.f / 640)) + 1e-5f))));
  rs[1] = __builtin_amdgcn_rcpf(rbf(__builtin_amdgcn_sqrtf(rbf(rbf(t1 * (1.f / 640)) + 1e-5f))));
  ms[0] = -rbf(m0) * rs[0]; ms[1] = -rbf(m1) * rs[1];
}
DI u32 mod_addr(u32 vec, u32 d) {
  return vec < 2 ? L_MSC + (d + 16 * (d >> 7)) * 8 + (vec == 0 ? 4 : 0) : L_M3 + d * 16 + (vec == 2 ? 0 : vec == 4 ? 4 : vec == 3 ? 8 : 12);
}
DI void mod_scatter(u32 gidx, u32 pay) {
  const u32 vec = gidx / 320, d = 2 * (gidx - vec * 320), a = mod_addr(vec, d);
  float lo = bflo(pay), hi = bfhi(pay);
  if (vec == 1 || vec == 4) { lo = rbf(1.f + lo); hi = rbf(1.f + hi); }
  lds_st<float>(a, lo); lds_st<float>(a + (vec < 2 ? 8 : 16), hi);
}

#define STAMP(i) do {} while (0)
#define DLY1 8
#define DLY2 16

__global__ void __launch_bounds__(256, 1) fused_kernel(const void* px, const void* pwr, const void* pwi, const void* pwg, const void* pwu, const void* pwd,
                                                       const void* pc, const void* pwm, void* pout, void* pws) {
  extern __shared__ __attribute__((aligned(16))) char smem[];
  STAMP(0);
  const u32 tid = threadIdx.x & 255, lane = tid & 63, l15 = lane & 15, g = lane >> 4;
  const u32 w = __builtin_amdgcn_readfirstlane(tid >> 6);
  const u32 b = blockIdx.x, xcd = b & 7, rr = b >> 3, pi = 32 * xcd + rr;
  const u32 mt = 4 * (xcd >> 1) + (rr >> 3), nt = 8 * (xcd & 1) + (rr & 7), half = xcd & 1, ct = rr & 7;
  const rsrc_t rX = __builtin_amdgcn_make_buffer_rsrc((void*)px, 0, 2048 * 640 * 2, 0x00020000);
  const rsrc_t rWr = __builtin_amdgcn_make_buffer_rsrc((void*)pwr, 0, 2048 * 321 * 2, 0x00020000);
  const rsrc_t rWi = __builtin_amdgcn_make_buffer_rsrc((void*)pwi, 0, 2048 * 321 * 2, 0x00020000);
  const rsrc_t rWg = __builtin_amdgcn_make_buffer_rsrc((void*)pwg, 0, 640 * 1536 * 2, 0x00020000);
  const rsrc_t rWu = __builtin_amdgcn_make_buffer_rsrc((void*)pwu, 0, 640 * 1536 * 2, 0x00020000);
  const rsrc_t rWd = __builtin_amdgcn_make_buffer_rsrc((void*)pwd, 0, 640 * 1536 * 2, 0x00020000);
  const rsrc_t rC = __builtin_amdgcn_make_buffer_rsrc((void*)pc, 0, 640 * 2, 0x00020000);
  const rsrc_t rWm = __builtin_amdgcn_make_buffer_rsrc((void*)pwm, 0, 640 * 3840 * 2, 0x00020000);
  const rsrc_t rO = __builtin_amdgcn_make_buffer_rsrc(pout, 0, 2048 * 640 * 2, 0x00020000);
  const rsrc_t rS = __builtin_amdgcn_make_buffer_rsrc(pws, 0, WS_USED, 0x00020000);
  u32 fail = 0;

  const u32 tagv = ldg1(rS, OFF_EPOCH, 1);
  u32 cv[3]; u32x4 wa[3], wb[3], xr[3];
#pragma unroll
  for (int j = 0; j < 3; ++j) {
    const u32 k = tid + 256 * j;
    cv[j] = __builtin_amdgcn_raw_buffer_load_b16(rC, k * 2, 0, 0);
    wa[j] = ldg4(rWm, k < 640 ? k * 7680 + pi * 16 : OOB, 0);
  }
#pragma unroll
  for (int j = 0; j < 3; ++j) {
    const u32 i = lane + 64 * j, e = i >= 80, ch = i - 80 * e;
    xr[j] = ldg4(rX, i < 160 ? (pi + 256 * (2 * w + e)) * 1280 + ch * 16 : OOB, 0);
  }
  const u32x4 ones = {~0u, ~0u, ~0u, ~0u};
#pragma unroll
  for (int j = 0; j < 3; ++j) { const u32 i = tid + 256 * j; stg4(rS, i < 704 ? OFF_E1 + pi * E1P + i * 16 : OOB, ones); }
  asm volatile("" ::: "memory");
  u32x4 pg[2], pu[2], pd[2]; u32 pl1[2], pl2[2];
#pragma unroll
  for (int j = 0; j < 2; ++j) {
    const u32 t = tid + 256 * j, ok = t < 480;
    const u32 row = t / 12, cb = t - row * 12, q = 5 * mt + (row >> 3), jj = row & 7;
    const u32 d = q < 64 ? 16 * (8 * (q >> 4) + jj) + (q & 15) : 16 * (32 + jj) + (q - 64);
    const u32 o = ok ? d * 3072 + nt * 192 + cb * 16 : OOB;
    pg[j] = ldg4(rWg, o, 0); pu[j] = ldg4(rWu, o, 0); pl1[j] = L_P1 + row * P1P + cb * 32;
    const u32 row2 = t / 10, cb2 = t - row2 * 10;
    pd[j] = ldg4(rWd, ok ? (48 * (2 * mt + half) + row2) * 1280 + ct * 160 + cb2 * 16 : OOB, 0); pl2[j] = L_P2 + row2 * P2P + cb2 * 16;
  }
  const u32 fe = (xcd + 8 * tid) * 321 + 10 * rr, fo = (fe & ~1u) * 2;
  u32x4 fr4 = ldg4(rWr, fo, 0), fi4 = ldg4(rWi, fo, 0);
  u32x2 fr2 = ldg2(rWr, fo + 16, 0), fi2 = ldg2(rWi, fo + 16, 0);
#pragma unroll
  for (int j = 0; j < 3; ++j) { const u32 k = tid + 256 * j; wb[j] = ldg4(rWm, (k < 640 && pi < 224) ? k * 7680 + (256 + pi) * 16 : OOB, 0); }
#pragma unroll
  for (int j = 0; j < 3; ++j) stg4(rS, OFF_E2 + pi * E2P + (tid + 256 * j) * 16, ones);
#pragma unroll
  for (int j = 0; j < 6; ++j) stg4(rS, OFF_H + (mt * 16 + nt) * 24576 + (tid + 256 * j) * 16, ones);

  asm volatile("" ::: "memory");
  const float fl = (float)l15, fg4 = (float)(4 * g);
  u32x4 bm32r, bm32i, bs32r, bs32i; u32 bm16r, bm16i, bs16r, bs16i;
  {
    const float ks = (float)(16 + (l15 & 7));
    float cm[10], sm[10], cs[10], ss[10];
#pragma unroll
    for (int i = 0; i < 10; ++i) {
      const float d1 = i < 8 ? (float)(8 * g + i) : (float)(32 + 2 * g + i - 8);
      const float a = d1 * fl * (1.f / 40), c = d1 * ks * (1.f / 40);
      cm[i] = cosr(a); sm[i] = -sinr(a); cs[i] = cosr(c); ss[i] = -sinr(c);
    }
#pragma unroll
    for (int j = 0; j < 4; ++j) {
      bm32r[j] = cvtpk(cm[2 * j], cm[2 * j + 1]); bm32i[j] = cvtpk(sm[2 * j], sm[2 * j + 1]);
      bs32r[j] = cvtpk(cs[2 * j], cs[2 * j + 1]); bs32i[j] = cvtpk(ss[2 * j], ss[2 * j + 1]);
    }
    bm16r = cvtpk(cm[8], cm[9]); bm16i = cvtpk(sm[8], sm[9]); bs16r = cvtpk(cs[8], cs[9]); bs16i = cvtpk(ss[8], ss[9]);
  }
  f32x4 twc, tws, tsc, tss, t2c, t2s;
  u32x4 f16r, f16i, i16r, i16i, k1r, k1i;
  {
    const float ks = (float)(16 + (l15 & 7));
    const float k2 = l15 < 8 ? fl : (float)((l15 & 12) + 3 - (l15 & 3)), sg = l15 < 8 ? 1.f : -1.f;
#pragma unroll
    for (int r = 0; r < 4; ++r) {
      const float bb = fg4 + r;
      twc[r] = cosr(bb * fl * (1.f / 640)); tws[r] = sinr(bb * fl * (1.f / 640));
      tsc[r] = cosr(bb * ks * (1.f / 640)); tss[r] = sinr(bb * ks * (1.f / 640));
      t2c[r] = cosr(bb * fl * (1.f / 256)); t2s[r] = sinr(bb * fl * (1.f / 256));
      const float c = cosr(bb * fl * (1.f / 16)), s = sinr(bb * fl * (1.f / 16));
      f16r[r] = cvtpk(c, s); f16i[r] = cvtpk(-s, c); i16r[r] = cvtpk(c, -s); i16i[r] = cvtpk(s, c);
      const float c1 = cosr(bb * k2 * (1.f / 16)), s1 = sinr(bb * k2 * (1.f / 16));
      k1r[r] = cvtpk(c1, s1); k1i[r] = cvtpk(-s1 * sg, c1 * sg);
    }
  }
  u32x2 m8f, m8i;
#pragma unroll
  for (int e = 0; e < 2; ++e) {
    const float a = (float)((l15 >> 1) * (256 * (2 * g + e) + pi)) * (1.f / 2048), c = cosr(a - floorf(a)), s = sinr(a - floorf(a));
    m8f[e] = (l15 & 1) ? cvtpk(-s, c) : cvtpk(c, s);
    const float a2 = (float)((2 * g + e) * (256 * (l15 >> 1) + pi)) * (1.f / 2048), c2 = cosr(a2 - floorf(a2)), s2 = sinr(a2 - floorf(a2));
    m8i[e] = (l15 & 1) ? cvtpk(s2, c2) : cvtpk(c2, -s2);
  }
  const u32 cmain = g < 2 ? l15 + 160 * g : 40 - l15 + 40 * (12 - 4 * g);
  const u32 k1s_ = 16 + (l15 & 7), k1c = k1s_ > 20 ? 20 : k1s_;
  const u32 cshr = g < 2 ? k1c + 160 * g : 40 - k1c + 40 * (12 - 4 * g);
  const u32 aMain = w * S3P + cmain * 8, aShrR = w * S3P + cshr * 8 + (l15 >> 3) * 4;
  const u32 aShrW = k1s_ > 20 ? 4 * S3P + lane * 4 : aShrR;

  const u32 tag = __builtin_amdgcn_readfirstlane(tagv) + 1;
  float sc[3];
#pragma unroll
  for (int j = 0; j < 3; ++j) { const float c = bflo(cv[j]); sc[j] = rbf(c * __builtin_amdgcn_rcpf(1.f + __expf(-c))); }
  {
    const f32x2 p = strip_reduce(wa, sc);
    if (l15 == 0) lds_st<f32x2>(L_RED + (w * 4 + g) * 8, p);
  }
#pragma unroll
  for (int j = 0; j < 3; ++j) { const u32 i = lane + 64 * j; if (i < 160) lds_st<u32x4>(L_XS + w * 2560 + i * 16, xr[j]); }
  WAIT_VM(22);
  STAMP(1);
  bar();
  if (tid < 4) {
    f32x2 p = lds_ld<f32x2>(L_RED + tid * 8);
#pragma unroll
    for (int j = 1; j < 4; ++j) p += lds_ld<f32x2>(L_RED + (j * 4 + tid) * 8);
    stg2(rS, OFF_MODG + (pi * 4 + tid) * 8, u32x2{cvtpk(p[0], p[1]), tag});
  }
  float xv[2][10], nv[2][10];
#pragma unroll
  for (int e = 0; e < 2; ++e)
#pragma unroll
    for (int i = 0; i < 10; ++i) {
      const u32 d1 = i < 8 ? 8 * g + i : 32 + 2 * g + i - 8;
      xv[e][i] = bflo(lds_ld<unsigned short>(L_XS + w * 2560 + e * 1280 + (16 * d1 + l15) * 2));
    }
  {
    float rs[2], ms[2]; ln_stats(xv, rs, ms);
#pragma unroll
    for (int e = 0; e < 2; ++e)
#pragma unroll
      for (int i = 0; i < 10; ++i) nv[e][i] = xv[e][i] * rs[e] + ms[e];
  }
#pragma unroll
  for (int j = 0; j < 2; ++j)
    if (tid + 256 * j < 480) {
      u32x4 lo, hi;
      lo[0] = __builtin_amdgcn_perm(pu[j][0], pg[j][0], 0x05040100); lo[1] = __builtin_amdgcn_perm(pu[j][0], pg[j][0], 0x07060302);
      lo[2] = __builtin_amdgcn_perm(pu[j][1], pg[j][1], 0x05040100); lo[3] = __builtin_amdgcn_perm(pu[j][1], pg[j][1], 0x07060302);
      hi[0] = __builtin_amdgcn_perm(pu[j][2], pg[j][2], 0x05040100); hi[1] = __builtin_amdgcn_perm(pu[j][2], pg[j][2], 0x07060302);
      hi[2] = __builtin_amdgcn_perm(pu[j][3], pg[j][3], 0x05040100); hi[3] = __builtin_amdgcn_perm(pu[j][3], pg[j][3], 0x07060302);
      lds_st<u32x4>(pl1[j], lo); lds_st<u32x4>(pl1[j] + 16, hi); lds_st<u32x4>(pl2[j], pd[j]);
    }
  {
    const u32 sh = (fe & 1) * 16;
    u32 r[6] = {fr4[0], fr4[1], fr4[2], fr4[3], fr2[0], fr2[1]}, im[6] = {fi4[0], fi4[1], fi4[2], fi4[3], fi2[0], fi2[1]};
    const u32 a = L_WL + ((tid & 15) * 20 + (tid >> 4)) * 4;
#pragma unroll
    for (int j = 0; j < 6; ++j) {
      const u32 rj = __builtin_amdgcn_alignbit(j < 5 ? r[j + 1] : 0u, r[j], sh), ij = __builtin_amdgcn_alignbit(j < 5 ? im[j + 1] : 0u, im[j], sh);
      lds_st<u32>(a + (2 * j) * KIP, __builtin_amdgcn_perm(ij, rj, 0x05040100));
      if (j < 5) lds_st<u32>(a + (2 * j + 1) * KIP, __builtin_amdgcn_perm(ij, rj, 0x07060302));
    }
  }
  {
    const f32x2 p = strip_reduce(wb, sc);
    if (l15 == 0) lds_st<f32x2>(L_RED + 128 + (w * 4 + g) * 8, p);
  }
  bar();
  if (tid < 4 && pi < 224) {
    f32x2 p = lds_ld<f32x2>(L_RED + 128 + tid * 8);
#pragma unroll
    for (int j = 1; j < 4; ++j) p += lds_ld<f32x2>(L_RED + 128 + (j * 4 + tid) * 8);
    stg2(rS, OFF_MODG + ((256 + pi) * 4 + tid) * 8, u32x2{cvtpk(p[0], p[1]), tag});
  }
  {
    const u32 q4 = l15 >> 2, p = l15 & 3;
    u32x2 t1[4][2], t2[2][2]; u32 o1[4], o2[2];
#pragma unroll
    for (int j = 0; j < 4; ++j) {
      const u32 k0 = 4 * (w + 4 * j) + g, k = k0 < 60 ? k0 : 59, ci = k / 12, ti = k - ci * 12, q = 5 * mt + ci, wq = ti / 3;
      const u32 a = L_P1 + (8 * ci + q4) * P1P + ti * 32 + p * 8;
      t1[j][0] = tr_read(a); t1[j][1] = tr_read(a + 4 * P1P);
      o1[j] = k0 < 60 ? OFF_WG1 + ((nt * 4 + wq) * 60 + (q >> 2) * 3 + (ti - wq * 3)) * 1024 + ((q & 3) * 16 + l15) * 16 : OOB;
    }
#pragma unroll
    for (int j = 0; j < 2; ++j) {
      const u32 k0 = 4 * (w + 4 * j) + g, k = k0 < 30 ? k0 : 29, ci = k / 5, ti = k - ci * 5, Q = 6 * (2 * mt + half) + ci, KS = Q >> 2, kq = KS / 12;
      const u32 a = L_P2 + (8 * ci + q4) * P2P + (ti < 4 ? (32 * (ti >> 1) + 8 * p + 4 * (ti & 1)) * 2 : (64 + 4 * p) * 2);
      t2[j][0] = tr_read(a); t2[j][1] = tr_read(a + 4 * P2P);
      o2[j] = k0 < 30 ? OFF_WG2 + ((ct * 4 + kq) * 60 + (KS - kq * 12) * 5 + ti) * 1024 + ((Q & 3) * 16 + l15) * 16 : OOB;
    }
    asm volatile("s_waitcnt lgkmcnt(0)" : "+v"(t1[0][0]), "+v"(t1[0][1]), "+v"(t1[1][0]), "+v"(t1[1][1]), "+v"(t1[2][0]), "+v"(t1[2][1]), "+v"(t1[3][0]), "+v"(t1[3][1]),
                 "+v"(t2[0][0]), "+v"(t2[0][1]), "+v"(t2[1][0]), "+v"(t2[1][1]) :: "memory");
#pragma unroll
    for (int j = 0; j < 4; ++j) stg4(rS, o1[j], u32x4{t1[j][0][0], t1[j][0][1], t1[j][1][0], t1[j][1][1]});
#pragma unroll
    for (int j = 0; j < 2; ++j) stg4(rS, o2[j], u32x4{t2[j][0][0], t2[j][0][1], t2[j][1][0], t2[j][1][1]});
  }
  const u32 u0 = w < 3 ? 3 * w : 8, u2 = w < 2 ? u0 + 2 : 10;
  f32x4 wfr[3], wfi[3];
#pragma unroll
  for (int j = 0; j < 3; ++j) {
    const u32x4 v = lds_ld<u32x4>(L_WL + (j < 2 ? u0 + j : u2) * KIP + l15 * 80 + g * 16);
#pragma unroll
    for (int r = 0; r < 4; ++r) { wfr[j][r] = bflo(v[r]); wfi[j][r] = bfhi(v[r]); }
  }
  STAMP(2);
  {
    u32x2 gr[4];
    for (u32 sp = 0;; ++sp) { asm volatile("" ::: "memory");
#pragma unroll
      for (int j = 0; j < 4; ++j) gr[j] = ldg2(rS, OFF_MODG + (tid + 256 * j) * 8, 1);
      if (__all(gr[0][1] == tag && gr[1][1] == tag && gr[2][1] == tag && gr[3][1] == tag)) break;
      if (sp > SPIN_CAP) { fail = 1; break; }
    }
#pragma unroll
    for (int j = 0; j < 4; ++j) mod_scatter(tid + 256 * j, gr[j][0]);
  }
  bar();
  STAMP(3);
  if (tid == 0) { stg1(rS, OFF_PREPF + b * 64, tag); if (b == 0) stg1(rS, OFF_EPOCH, tag); }

  {
    u32x4 a32[2]; u32x2 a16[2];
    {
      float h[2][10];
#pragma unroll
      for (int i = 0; i < 10; ++i) {
        const u32 d1 = i < 8 ? 8 * g + i : 32 + 2 * g + i - 8, d = 16 * d1 + l15;
        const f32x2 m = lds_ld<f32x2>(L_MSC + (d + 16 * (d >> 7)) * 8);
        h[0][i] = nv[0][i] * m[0] + m[1]; h[1][i] = nv[1][i] * m[0] + m[1];
      }
#pragma unroll
      for (int e = 0; e < 2; ++e) {
        a32[e] = u32x4{cvtpk(h[e][0], h[e][1]), cvtpk(h[e][2], h[e][3]), cvtpk(h[e][4], h[e][5]), cvtpk(h[e][6], h[e][7])};
        a16[e] = u32x2{cvtpk(h[e][8], h[e][9]), 0u};
      }
    }
    f32x4 cr[3], ci[3];
    {
      const bool lo = l15 < 8; const u32x4 z4 = {0, 0, 0, 0};
      const u32x4 s0r = lo ? bs32r : z4, s1r = lo ? z4 : bs32r, s0i = lo ? bs32i : z4, s1i = lo ? z4 : bs32i;
      const u32x2 t0r = {lo ? bs16r : 0u, 0u}, t1r = {lo ? 0u : bs16r, 0u}, t0i = {lo ? bs16i : 0u, 0u}, t1i = {lo ? 0u : bs16i, 0u};
      const u32x2 m16r = {bm16r, 0u}, m16i = {bm16i, 0u};
      asm volatile("s_nop 1\n\t"
                   M32 "%0, %6, %10, 0\n\t" M32 "%3, %6, %11, 0\n\t" M32 "%1, %7, %10, 0\n\t" M32 "%4, %7, %11, 0\n\t"
                   M32 "%2, %6, %14, 0\n\t" M32 "%5, %6, %16, 0\n\t"
                   M16 "%0, %8, %12, %0\n\t" M16 "%3, %8, %13, %3\n\t" M16 "%1, %9, %12, %1\n\t" M16 "%4, %9, %13, %4\n\t"
                   M32 "%2, %7, %15, %2\n\t" M32 "%5, %7, %17, %5\n\t"
                   M16 "%2, %8, %18, %2\n\t" M16 "%5, %8, %20, %5\n\t" M16 "%2, %9, %19, %2\n\t" M16 "%5, %9, %21, %5\n\ts_nop 7"
                   : "=&v"(cr[0]), "=&v"(cr[1]), "=&v"(cr[2]), "=&v"(ci[0]), "=&v"(ci[1]), "=&v"(ci[2])
                   : "v"(a32[0]), "v"(a32[1]), "v"(a16[0]), "v"(a16[1]), "v"(bm32r), "v"(bm32i), "v"(m16r), "v"(m16i),
                     "v"(s0r), "v"(s1r), "v"(s0i), "v"(s1i), "v"(t0r), "v"(t1r), "v"(t0i), "v"(t1i));
    }
    u32x4 dd[3] = {cmul_pack<-1>(cr[0], ci[0], twc, tws), cmul_pack<-1>(cr[1], ci[1], twc, tws), cmul_pack<-1>(cr[2], ci[2], tsc, tss)};
    mm3<false>(cr, ci, dd, k1r, k1i);
#pragma unroll
    for (int r = 0; r < 4; ++r) {
      lds_st<u32x2>(L_S3 + aMain + r * 320, u32x2{cvtpk(cr[0][r], ci[0][r]), cvtpk(cr[1][r], ci[1][r])});
      lds_st<u32>(L_S3 + aShrW + r * 320, cvtpk(cr[2][r], ci[2][r]));
    }
    bar();
    u32x2 d8[6]; f32x4 o8[6];
    {
      const u32 a = L_S3 + g * S3P + w * 640;
      const u32x4 v0 = lds_ld<u32x4>(a + l15 * 32), v1 = lds_ld<u32x4>(a + l15 * 32 + 16);
      d8[0] = u32x2{v0[0], v0[1]}; d8[1] = u32x2{v0[2], v0[3]}; d8[2] = u32x2{v1[0], v1[1]}; d8[3] = u32x2{v1[2], v1[3]};
      d8[4] = lds_ld<u32x2>(a + 512 + l15 * 8); d8[5] = lds_ld<u32x2>(L_S3 + g * S3P + 2560 + l15 * 8);
    }
    mm6k16(o8, m8f, d8);
#pragma unroll
    for (int k = 0; k < 2; ++k) {
      const u32 o = OFF_E1 + pi * E1P + (2 * g + k) * E1R + w * 320;
      u32 v[6];
#pragma unroll
      for (int t = 0; t < 6; ++t) v[t] = nosent(cvtpk(o8[t][2 * k], o8[t][2 * k + 1]));
      stg4(rS, o + l15 * 16, u32x4{v[0], v[1], v[2], v[3]});
      stg1(rS, o + 256 + l15 * 4, v[4]);
      stg1(rS, (w == 3 && l15 == 0) ? o + 320 : OOB, v[5]);
    }
  }
  STAMP(4);
  u32x4 iar, iai, ga_[3], gs_[3]; f32x4 usc, uss;
  {
#pragma unroll
    for (int j = 0; j < 4; ++j) {
      const float k2 = g < 2 ? fg4 + j : fg4 + 3 - j, sg = g < 2 ? 1.f : -1.f;
      const float c = cosr(k2 * fl * (1.f / 16)), s = sinr(k2 * fl * (1.f / 16));
      iar[j] = cvtpk(c, -s * sg); iai[j] = cvtpk(s, c * sg);
      const float ks = (float)(16 + ((4 * g + j) & 7));
      usc[j] = cosr(ks * fl * (1.f / 640)); uss[j] = sinr(ks * fl * (1.f / 640));
    }
#pragma unroll
    for (int t = 0; t < 3; ++t) {
      const float d1 = t < 2 ? (float)(8 * (l15 >> 2) + 4 * t + (l15 & 3)) : (float)(32 + 2 * (l15 >> 2) + (l15 & 3));
#pragma unroll
      for (int j = 0; j < 4; ++j) {
        const float k1 = fg4 + j, wm = (g == 0 && j == 0) ? 1.f : 2.f;
        ga_[t][j] = cvtpk(wm * cosr(d1 * k1 * (1.f / 40)), -wm * sinr(d1 * k1 * (1.f / 40)));
        const u32 kk = 16 + 4 * (g & 1) + j; const float wsv = kk < 20 ? 2.f : kk == 20 ? 1.f : 0.f, kf = (float)kk;
        gs_[t][j] = cvtpk(wsv * cosr(d1 * kf * (1.f / 40)), -wsv * sinr(d1 * kf * (1.f / 40)));
      }
    }
  }
  const u32 wv = lane * 16;
  const u32 rot = ((nt & 7) + 3 * (mt & 3)) % 10, rot2 = ct & 3;
  const u32 w1base = OFF_WG1 + (nt * 4 + w) * 61440, w2base = OFF_WG2 + (ct * 4 + w) * 61440;

  {
    u32 go[5], lo[5];
#pragma unroll
    for (int j = 0; j < 5; ++j) {
      const u32 idx = tid + 256 * j, s2 = idx / 5, e = idx - 5 * s2;
      go[j] = OFF_E1 + s2 * E1P + xcd * E1R + rr * 40 + e * 8;
      lo[j] = L_KI + 2 * e * KIP + ((s2 & 15) * 20 + (s2 >> 4)) * 4;
    }
    const u32 gx = rr == 31 ? OFF_E1 + tid * E1P + xcd * E1R + 1280 : OOB;
    {
      const u32 fo2 = OFF_PREPF + (((lane >> 2) & 7) + 8 * (8 * (lane & 3) + ct)) * 64;
      for (u32 sp = 0;; ++sp) { asm volatile("" ::: "memory"); if (__all(ldg1(rS, fo2, 1) == tag)) break; if (sp > SPIN_CAP || fail) { fail = 1; break; } }
    }
    __builtin_amdgcn_s_sleep(DLY1);
    u32x2 v[5]; u32 vx;
    for (u32 sp = 0;; ++sp) { asm volatile("" ::: "memory");
#pragma unroll
      for (int j = 0; j < 5; ++j) v[j] = ldg2(rS, go[j], 1);
      vx = ldg1(rS, gx, 1);
      u32 m = vx;
#pragma unroll
      for (int j = 0; j < 5; ++j) m = max3u(m, v[j][0], v[j][1]);
      if (__all(m != 0xffffffffu)) break;
      if (sp > SPIN_CAP || fail) { fail = 1; break; }
    }
    STAMP(5);
#pragma unroll
    for (int j = 0; j < 5; ++j) { lds_st<u32>(lo[j], v[j][0]); lds_st<u32>(lo[j] + KIP, v[j][1]); }
    lds_st<u32>(L_KI + 10 * KIP + ((tid & 15) * 20 + (tid >> 4)) * 4, vx);
    asm volatile("s_nop 4" ::: "memory");
    load_w1_slot<0>(wv, rS, w1base, rot); load_w1_slot<1>(wv, rS, w1base, rot); load_w1_slot<2>(wv, rS, w1base, rot);
    load_w1_slot<3>(wv, rS, w1base, rot); load_w1_slot<4>(wv, rS, w1base, rot);
    bar();
    u32x4 dd[3]; f32x4 cr[3], ci[3];
#pragma unroll
    for (int j = 0; j < 3; ++j) dd[j] = lds_ld<u32x4>(L_KI + (j < 2 ? u0 + j : u2) * KIP + l15 * 80 + g * 16);
    mm3<true>(cr, ci, dd, f16r, f16i);
#pragma unroll
    for (int j = 0; j < 3; ++j) dd[j] = cmul_pack<-1>(cr[j], ci[j], t2c, t2s);
    mm3<false>(cr, ci, dd, f16r, f16i);
#pragma unroll
    for (int j = 0; j < 3; ++j) dd[j] = cmul_pack<1>(cr[j], ci[j], wfr[j], wfi[j]);
    mm3<true>(cr, ci, dd, i16r, i16i);
#pragma unroll
    for (int j = 0; j < 3; ++j) dd[j] = cmul_pack<1>(cr[j], ci[j], t2c, t2s);
    mm3<false>(cr, ci, dd, i16r, i16i);
    const u32 ao = L_KO + (64 * g + l15) * 40 + u0 * 4;
#pragma unroll
    for (int r = 0; r < 4; ++r) {
      lds_st<u32>(ao + r * 640, nosent(cvtpk(cr[0][r], ci[0][r])));
      lds_st<u32>(ao + r * 640 + 4, nosent(cvtpk(cr[1][r], ci[1][r])));
      const u32 v2 = nosent(cvtpk(cr[2][r], ci[2][r]));
      if (w < 2) lds_st<u32>(ao + r * 640 + 8, v2);
      else if (w == 2 && rr == 31) stg2(rS, OFF_E2 + pi * E2P + 10240 + (64 * g + 16 * r + l15) * 8, u32x2{v2, v2});
    }
    bar();
#pragma unroll
    for (int j = 0; j < 3; ++j) {
      const u32 i = tid + 256 * j;
      if (i < 640) stg4(rS, OFF_E2 + pi * E2P + i * 16, lds_ld<u32x4>(L_KO + i * 16));
    }
  }
  STAMP(6);
  {
    u32x2 gr[4];
    for (u32 sp = 0;; ++sp) { asm volatile("" ::: "memory");
#pragma unroll
      for (int j = 0; j < 4; ++j) gr[j] = ldg2(rS, OFF_MODG + (1024 + (tid + 256 * j < 896 ? tid + 256 * j : tid)) * 8, 1);
      if (__all(gr[0][1] == tag && gr[1][1] == tag && gr[2][1] == tag && gr[3][1] == tag)) break;
      if (sp > SPIN_CAP || fail) { fail = 1; break; }
    }
#pragma unroll
    for (int j = 0; j < 4; ++j) if (tid + 256 * j < 896) mod_scatter(1024 + tid + 256 * j, gr[j][0]);
  }

  const u32 tau0 = 128 * mt + 8 * nt + 2 * w;
  float gbv[20];
  {
    u32 go[3][2], lw[3];
#pragma unroll
    for (int i = 0; i < 3; ++i) {
      const u32 p0 = 16 * i + l15, ext = (p0 == 40 && w == 3), p = p0 < 40 ? p0 : l15, c0 = 80 * w + 2 * p, cg = c0 / 10;
#pragma unroll
      for (int e = 0; e < 2; ++e)
        go[i][e] = OFF_E2 + (32 * (2 * g + e) + (ext ? 31 : cg)) * E2P + (ext ? 10240 + 8 * pi : 40 * pi + 4 * (c0 - 10 * cg));
      lw[i] = (p0 < 40 || ext) ? L_S3B + g * S3P + (80 * w + 2 * p0) * 8 : L_S3BD + lane * 16;
    }
    u32x2 v[3][2];
    for (u32 sp = 0;; ++sp) { asm volatile("" ::: "memory");
#pragma unroll
      for (int i = 0; i < 3; ++i) { v[i][0] = ldg2(rS, go[i][0], 1); v[i][1] = ldg2(rS, go[i][1], 1); }
      u32 m = 0;
#pragma unroll
      for (int i = 0; i < 3; ++i) { m = max3u(m, v[i][0][0], v[i][0][1]); m = max3u(m, v[i][1][0], v[i][1][1]); }
      if (__all(m != 0xffffffffu)) break;
      if (sp > SPIN_CAP || fail) { fail = 1; break; }
    }
    STAMP(7);
    u32x2 d8[6]; f32x4 o8[6];
#pragma unroll
    for (int i = 0; i < 3; ++i) { d8[2 * i] = u32x2{v[i][0][0], v[i][1][0]}; d8[2 * i + 1] = u32x2{v[i][0][1], v[i][1][1]}; }
    mm6k16(o8, m8i, d8);
#pragma unroll
    for (int i = 0; i < 3; ++i)
      lds_st<u32x4>(lw[i], u32x4{cvtpk(o8[2 * i][0], o8[2 * i][1]), cvtpk(o8[2 * i][2], o8[2 * i][3]), cvtpk(o8[2 * i + 1][0], o8[2 * i + 1][1]), cvtpk(o8[2 * i + 1][2], o8[2 * i + 1][3])});
    bar();
    u32x4 dd[3]; f32x4 cr[3], ci[3];
#pragma unroll
    for (int j = 0; j < 4; ++j) {
      const u32x2 t = lds_ld<u32x2>(L_S3B + aMain + j * 320);
      dd[0][j] = t[0]; dd[1][j] = t[1]; dd[2][j] = lds_ld<u32>(L_S3B + aShrR + j * 320);
    }
    f32x4 m3[10];
#pragma unroll
    for (int i = 0; i < 10; ++i) { const u32 d1 = i < 8 ? 8 * g + i : 32 + 2 * g + i - 8; m3[i] = lds_ld<f32x4>(L_M3 + (16 * d1 + l15) * 16); }
#pragma unroll
    for (int i = 0; i < 5; ++i)
#pragma unroll
      for (int r = 0; r < 4; ++r) gbv[4 * i + r] = lds_ld<float>(L_M3 + (80 * ct + (i < 4 ? 32 * (i >> 1) + 8 * g + 4 * (i & 1) : 64 + 4 * g) + r) * 16 + 12);
    mm3<true>(cr, ci, dd, iar, iai);
    dd[0] = cmul_pack<1>(cr[0], ci[0], twc, tws); dd[1] = cmul_pack<1>(cr[1], ci[1], twc, tws); dd[2] = cmul_pack<1>(cr[2], ci[2], usc, uss);
    f32x4 y[2][3];
    {
      const bool lo = g < 2; const u32x4 z4 = {0, 0, 0, 0};
      const u32x4 s00 = lo ? gs_[0] : z4, s01 = lo ? gs_[1] : z4, s02 = lo ? gs_[2] : z4, s10 = lo ? z4 : gs_[0], s11 = lo ? z4 : gs_[1], s12 = lo ? z4 : gs_[2];
      asm volatile("s_nop 1\n\t"
                   M32 "%0, %9, %6, 0\n\t" M32 "%1, %10, %6, 0\n\t" M32 "%2, %11, %6, 0\n\t" M32 "%3, %9, %7, 0\n\t" M32 "%4, %10, %7, 0\n\t" M32 "%5, %11, %7, 0\n\t"
                   M32 "%0, %12, %8, %0\n\t" M32 "%1, %13, %8, %1\n\t" M32 "%2, %14, %8, %2\n\t" M32 "%3, %15, %8, %3\n\t" M32 "%4, %16, %8, %4\n\t" M32 "%5, %17, %8, %5\n\ts_nop 7"
                   : "=&v"(y[0][0]), "=&v"(y[0][1]), "=&v"(y[0][2]), "=&v"(y[1][0]), "=&v"(y[1][1]), "=&v"(y[1][2])
                   : "v"(dd[0]), "v"(dd[1]), "v"(dd[2]), "v"(ga_[0]), "v"(ga_[1]), "v"(ga_[2]), "v"(s00), "v"(s01), "v"(s02), "v"(s10), "v"(s11), "v"(s12));
    }
    float x1[2][10];
#pragma unroll
    for (int e = 0; e < 2; ++e)
#pragma unroll
      for (int i = 0; i < 10; i += 2) {
        const float g0 = m3[i][0] * (1.f / (2048.f * 640.f)), g1 = m3[i + 1][0] * (1.f / (2048.f * 640.f));
        const u32 p = cvtpk(xv[e][i] + g0 * y[e][i >> 2][i & 3], xv[e][i + 1] + g1 * y[e][i >> 2][(i & 3) + 1]);
        x1[e][i] = bflo(p); x1[e][i + 1] = bfhi(p);
        const u32 d1 = i < 8 ? 8 * g + i : 32 + 2 * g, a = L_XT + w * 2560 + e * 1280 + (16 * d1 + l15) * 2;
        lds_st<unsigned short>(a, (unsigned short)p); lds_st<unsigned short>(a + 32, (unsigned short)(p >> 16));
      }
    float rs[2], ms[2]; ln_stats(x1, rs, ms);
#pragma unroll
    for (int e = 0; e < 2; ++e) {
      float h[10];
#pragma unroll
      for (int i = 0; i < 10; ++i) h[i] = (x1[e][i] * rs[e] + ms[e]) * m3[i][1] + m3[i][2];
      const u32 o = OFF_H2 + (tau0 + e) * 1280;
      stg4(rS, o + lane * 16, u32x4{cvtpk(h[0], h[1]), cvtpk(h[2], h[3]), cvtpk(h[4], h[5]), cvtpk(h[6], h[7])});
      stg1(rS, o + 1024 + l15 * 16 + g * 4, cvtpk(h[8], h[9]));
    }
#pragma unroll
    for (int j = 0; j < 3; ++j) {
      const u32 i = lane + 64 * j;
      if (i < 160) stg4(rS, OFF_X1 + tau0 * 1280 + i * 16, lds_ld<u32x4>(L_XT + w * 2560 + i * 16));
    }
    WAIT_VM(0);
    if (lane == 0) stg1(rS, OFF_H2F + (mt * 16 + nt) * 64 + w * 4, tag);
  }
  STAMP(8);
  asm volatile("s_nop 4" ::: "memory");
  load_w1_slot<5>(wv, rS, w1base, rot); load_w1_slot<6>(wv, rS, w1base, rot); load_w1_slot<7>(wv, rS, w1base, rot);
  load_w1_slot<8>(wv, rS, w1base, rot); load_w1_slot<9>(wv, rS, w1base, rot);
  bar();
  {
    const u32 fo2 = OFF_H2F + (mt * 16 + (lane >> 2)) * 64 + (lane & 3) * 4;
    for (u32 sp = 0;; ++sp) { asm volatile("" ::: "memory"); if (__all(ldg1(rS, fo2, 1) == tag)) break; if (sp > SPIN_CAP || fail) { fail = 1; break; } }
  }
  STAMP(9);
  const u32 tl = 64 * half + 16 * w + l15;
  u32x4 xa, xb; u32x2 xc;
  {
    const u32 o = OFF_X1 + (128 * mt + tl) * 1280 + ct * 160;
    xa = ldg4(rS, o + g * 16, 1); xb = ldg4(rS, o + 64 + g * 16, 1); xc = ldg2(rS, o + 128 + g * 8, 1);
    asm volatile("" : "+v"(xa), "+v"(xb), "+v"(xc));
  }
  G1 s;
  {
    const u32 tok = lane >> 3, dv = OFF_H2 + (128 * mt + 32 * w + tok) * 1280 + (((lane & 7) ^ tok) * 16);
    asm volatile("s_nop 4" ::: "memory");
#pragma unroll
    for (int c = 0; c < 10; ++c) {
      u32 cc = c + rot; cc = cc >= 10 ? cc - 10 : cc;
#pragma unroll
      for (int t = 0; t < 4; ++t) dma16(dv, rS, cc * 128 + t * 10240, c * 16384 + (4 * w + t) * 1024);
    }
    const u32 tk = l15 & 7;
#pragma unroll
    for (int h = 0; h < 2; ++h)
#pragma unroll
      for (int j = 0; j < 3; ++j) s.ad[h][j] = j * 65536 + (l15 >> 3) * 1024 + tk * 128 + (((4 * h + g) ^ tk) * 16);
    asm volatile("s_waitcnt vmcnt(36)\n\ts_barrier" ::: "memory");
    STAMP(10);
#pragma unroll
    for (int t = 0; t < 8; ++t) asm volatile("ds_read_b128 %0, %1 offset:%c2" : "=v"(s.fr[t]) : "v"(s.ad[0][0]), "i"(t * 2048) : "memory");
  }
  g1_all(s, wv, rS, w2base, rot2, std::make_integer_sequence<int, 20>{});
  load_w2<54>(wv, rS, w2base, rot2); load_w2<55>(wv, rS, w2base, rot2); load_w2<56>(wv, rS, w2base, rot2);
  load_w2<57>(wv, rS, w2base, rot2); load_w2<58>(wv, rS, w2base, rot2); load_w2<59>(wv, rS, w2base, rot2);
  asm volatile("s_nop 7" : "+v"(s.acc[0][0]), "+v"(s.acc[0][1]), "+v"(s.acc[0][2]), "+v"(s.acc[1][0]), "+v"(s.acc[1][1]), "+v"(s.acc[1][2]), "+v"(s.acc[2][0]), "+v"(s.acc[2][1]),
               "+v"(s.acc[2][2]), "+v"(s.acc[3][0]), "+v"(s.acc[3][1]), "+v"(s.acc[3][2]), "+v"(s.acc[4][0]), "+v"(s.acc[4][1]), "+v"(s.acc[4][2]), "+v"(s.acc[5][0]),
               "+v"(s.acc[5][1]), "+v"(s.acc[5][2]), "+v"(s.acc[6][0]), "+v"(s.acc[6][1]), "+v"(s.acc[6][2]), "+v"(s.acc[7][0]), "+v"(s.acc[7][1]), "+v"(s.acc[7][2]));
  STAMP(11);
#pragma unroll
  for (int hh = 0; hh < 2; ++hh)
#pragma unroll
    for (int i = 0; i < 3; ++i) {
      u32 p[4];
#pragma unroll
      for (int t = 0; t < 4; ++t) {
        const f32x4 a = s.acc[4 * hh + t][i];
        p[t] = nosent(cvtpk((a[0] > 0.f ? a[0] : a[0] * 0.f) * a[1], (a[2] > 0.f ? a[2] : a[2] * 0.f) * a[3]));
      }
      swap32u(p[0], p[2]); swap32u(p[1], p[3]); swap16u(p[0], p[1]); swap16u(p[2], p[3]);
      stg4(rS, OFF_H + (mt * 16 + nt) * 24576 + (4 * hh + g) * 3072 + (3 * w + i) * 256 + l15 * 16, u32x4{p[0], p[1], p[2], p[3]});
    }
  STAMP(12);
  f32x4 acc[4][5];
  {
    const u32 hb = OFF_H + (mt * 16 + 4 * w) * 24576 + 4 * half * 3072;
    const u32 hv[4] = {wv, wv + 3072, wv + 6144, wv + 9216};
    const u32 so0 = hb + ((0 + rot2) & 3) * 24576, so1 = hb + ((1 + rot2) & 3) * 24576, so2 = hb + ((2 + rot2) & 3) * 24576, so3 = hb + ((3 + rot2) & 3) * 24576;
    u32x4 hA[12], hB[12];
    __builtin_amdgcn_s_sleep(DLY2);
    h_load<true>(hA, rS, hv, so0); h_load<true>(hB, rS, hv, so1);
    g2_prod<0>(acc, hA, rS, hv, so0, fail);
    STAMP(13);
    h_load<false>(hA, rS, hv, so2);
    g2_prod<1>(acc, hB, rS, hv, so1, fail);
    h_load<false>(hB, rS, hv, so3);
    g2_prod<2>(acc, hA, rS, hv, so2, fail);
    g2_prod<3>(acc, hB, rS, hv, so3, fail);
  }
  asm volatile("s_nop 7" : "+v"(acc[0][0]), "+v"(acc[0][1]), "+v"(acc[0][2]), "+v"(acc[0][3]), "+v"(acc[0][4]), "+v"(acc[1][0]), "+v"(acc[1][1]), "+v"(acc[1][2]), "+v"(acc[1][3]),
               "+v"(acc[1][4]), "+v"(acc[2][0]), "+v"(acc[2][1]), "+v"(acc[2][2]), "+v"(acc[2][3]), "+v"(acc[2][4]), "+v"(acc[3][0]), "+v"(acc[3][1]), "+v"(acc[3][2]),
               "+v"(acc[3][3]), "+v"(acc[3][4]));
  STAMP(14);
  bar();
  f32x4 fin[5];
#pragma unroll
  for (int t = 0; t < 4; ++t) {
    if (t == (int)w) {
#pragma unroll
      for (int i = 0; i < 5; ++i) fin[i] = acc[t][i];
    } else {
      const u32 sl = (w > (u32)t ? w - 1 : w);
#pragma unroll
      for (int i = 0; i < 5; ++i) lds_st<f32x4>(((t * 3 + sl) * 5 + i) * 1024 + lane * 16, acc[t][i]);
    }
  }
  bar();
#pragma unroll
  for (int sl = 0; sl < 3; ++sl)
#pragma unroll
    for (int i = 0; i < 5; ++i) fin[i] += lds_ld<f32x4>(((w * 3 + sl) * 5 + i) * 1024 + lane * 16);
  {
    const u32 xs[10] = {xa[0], xa[1], xa[2], xa[3], xb[0], xb[1], xb[2], xb[3], xc[0], xc[1]};
    u32 o[10];
#pragma unroll
    for (int i = 0; i < 5; ++i)
#pragma unroll
      for (int r = 0; r < 4; r += 2) {
        const u32 x = xs[2 * i + (r >> 1)];
        float v0 = bflo(x) + gbv[4 * i + r] * fin[i][r], v1 = bfhi(x) + gbv[4 * i + r + 1] * fin[i][r + 1];
        if (fail) { v0 = __builtin_nanf(""); v1 = v0; }
        o[2 * i + (r >> 1)] = cvtpk(v0, v1);
      }
    const u32 row = 32 * xcd + 8 * (mt & 3) + ((tl >> 3) & 7) + 256 * (tl & 7), oo = row * 1280 + ct * 160;
    __builtin_amdgcn_raw_buffer_store_b128(u32x4{o[0], o[1], o[2], o[3]}, rO, oo + g * 16, 0, 0);
    __builtin_amdgcn_raw_buffer_store_b128(u32x4{o[4], o[5], o[6], o[7]}, rO, oo + 64 + g * 16, 0, 0);
    __builtin_amdgcn_raw_buffer_store_b64(u32x2{o[8], o[9]}, rO, oo + 128 + g * 8, 0, 0);
  }
}

extern "C" void kernel_launch(void* const* d_in, const int* in_sizes, int n_in, void* d_out, int out_size, void* d_ws, size_t ws_size, hipStream_t stream) {
  (void)hipFuncSetAttribute((const void*)fused_kernel, hipFuncAttributeMaxDynamicSharedMemorySize, LDS_BYTES);
  fused_kernel<<<256, 256, LDS_BYTES, stream>>>(d_in[0], d_in[1], d_in[2], d_in[4], d_in[5], d_in[6], d_in[7], d_in[8], d_out, d_ws);
}
```

```cpp
#include <hip/hip_runtime.h>
#include <hip/hip_bf16.h>
#include <utility>

typedef unsigned u32;
typedef unsigned u32x2 __attribute__((ext_vector_type(2)));
typedef unsigned u32x3 __attribute__((ext_vector_type(3)));
typedef unsigned u32x4 __attribute__((ext_vector_type(4)));
typedef float f32x2 __attribute__((ext_vector_type(2)));
typedef float f32x4 __attribute__((ext_vector_type(4)));
typedef __bf16 bf16x2 __attribute__((ext_vector_type(2)));
typedef __amdgpu_buffer_rsrc_t rsrc_t;
#define DI __device__ __forceinline__
#define LAS __attribute__((address_space(3)))
#define SC1 16

constexpr u32 OFF_EPOCH = 0, OFF_MODG = 0x1000, OFF_FLG = 0x5000;
constexpr u32 E1P = 11264, E1R = 1408, E2P = 12288;
constexpr u32 OFF_E1 = 0x10000, OFF_E2 = 0x2D0000, OFF_H2 = 0x5D0000, OFF_X1 = 0x850000, OFF_WG1 = 0xAD0000,
              OFF_WG2 = 0xE90000, OFF_H = 0x1070000, OFF_FW = 0x1670000, FWP = 13312, WS_USED = 0x19B0000, OFF_STAMP = 0x1F00000;
constexpr u32 OOB = 0x7ff00000u;
constexpr u32 L_TAB = 0, L_XS = 34816, L_RED = 45056, L_MSC = 45312, L_M3 = 51072, L_S3 = 61312, S3P = 2688, L_KI = 73600, KIP = 1328,
              L_WL = 89536, L_KO = 105472, L_P1 = 117760, P1P = 208, L_P2 = 134400, P2P = 176, L_FS = 142848, LDS_BYTES = 163840;
constexpr u32 SPIN_CAP = 40000;

enum { T_BM32R, T_BM32I, T_S0R, T_S1R, T_S0I, T_S1I, T_K16R, T_K16I, T_K16S, T_TWC, T_TWS, T_TSC, T_TSS, T_K1R, T_K1I,
       T_T2C, T_T2S, T_F16R, T_F16I, T_I16R, T_I16I, T_IAR, T_IAI, T_USC, T_USS, T_GA, T_GS0 = T_GA + 3, T_GS1 = T_GS0 + 3, T_N = T_GS1 + 3 };
struct Trig { double c[1280]; };
constexpr double ccos(double x) { double t = 1, s = 1; for (int i = 1; i < 14; ++i) { t *= -x * x / ((2 * i - 1) * (2 * i)); s += t; } return s; }
constexpr Trig make_trig() {
  Trig r{};
  for (int j = 0; j <= 320; ++j) {
    const double v = j == 320 ? 0.0 : ccos(2 * 3.14159265358979323846 * j / 1280);
    r.c[j] = v; r.c[640 - j] = -v; r.c[640 + j] = -v; if (j) r.c[1280 - j] = v;
  }
  return r;
}
constexpr Trig TR = make_trig();
constexpr double cs(int p, int n) { return TR.c[(p % n) * (1280 / n)]; }
constexpr double sn(int p, int n) { return TR.c[((p % n) * (1280 / n) + 960) % 1280]; }
constexpr u32 fb(double v) { return __builtin_bit_cast(u32, (float)v); }
constexpr u32 bfr(double v) { const u32 u = fb(v + 0.0); return (u + 0x7fffu + ((u >> 16) & 1)) >> 16; }
constexpr u32 pk(double a, double b) { return bfr(a) | (bfr(b) << 16); }
struct Tab { u32 v[T_N][64][4]; };
constexpr Tab make_tab() {
  Tab t{};
  for (int l = 0; l < 64; ++l) {
    const int n = l & 15, g = l >> 4, ks = 16 + (n & 7); const bool lo = n < 8;
    for (int j = 0; j < 4; ++j) {
      const int d = 8 * g + 2 * j, b = 4 * g + j;
      t.v[T_BM32R][l][j] = pk(cs(d * n, 40), cs((d + 1) * n, 40)); t.v[T_BM32I][l][j] = pk(-sn(d * n, 40), -sn((d + 1) * n, 40));
      const u32 sr = pk(cs(d * ks, 40), cs((d + 1) * ks, 40)), si = pk(-sn(d * ks, 40), -sn((d + 1) * ks, 40));
      t.v[T_S0R][l][j] = lo ? sr : 0; t.v[T_S1R][l][j] = lo ? 0 : sr; t.v[T_S0I][l][j] = lo ? si : 0; t.v[T_S1I][l][j] = lo ? 0 : si;
      t.v[T_TWC][l][j] = fb(cs(b * n, 640)); t.v[T_TWS][l][j] = fb(sn(b * n, 640));
      t.v[T_TSC][l][j] = fb(cs(b * ks, 640)); t.v[T_TSS][l][j] = fb(sn(b * ks, 640));
      const int k2 = lo ? n : (n & 12) + 3 - (n & 3); const double sg = lo ? 1 : -1;
      t.v[T_K1R][l][j] = pk(cs(b * k2, 16), sn(b * k2, 16)); t.v[T_K1I][l][j] = pk(-sn(b * k2, 16) * sg, cs(b * k2, 16) * sg);
      t.v[T_T2C][l][j] = fb(cs(b * n, 256)); t.v[T_T2S][l][j] = fb(sn(b * n, 256));
      const double c = cs(b * n, 16), s = sn(b * n, 16);
      t.v[T_F16R][l][j] = pk(c, s); t.v[T_F16I][l][j] = pk(-s, c); t.v[T_I16R][l][j] = pk(c, -s); t.v[T_I16I][l][j] = pk(s, c);
      const int q2 = g < 2 ? b : 4 * g + 3 - j; const double s2 = g < 2 ? 1 : -1;
      t.v[T_IAR][l][j] = pk(cs(q2 * n, 16), -sn(q2 * n, 16) * s2); t.v[T_IAI][l][j] = pk(sn(q2 * n, 16), cs(q2 * n, 16) * s2);
      t.v[T_USC][l][j] = fb(cs((16 + (b & 7)) * n, 640)); t.v[T_USS][l][j] = fb(sn((16 + (b & 7)) * n, 640));
      for (int i = 0; i < 3; ++i) {
        const int d1 = i < 2 ? 8 * (n >> 2) + 4 * i + (n & 3) : 32 + 2 * (n >> 2) + (n & 3), kk = 16 + 4 * (g & 1) + j;
        const double wm = b == 0 ? 1 : 2, ws = kk < 20 ? 2 : kk == 20 ? 1 : 0;
        t.v[T_GA + i][l][j] = pk(wm * cs(d1 * b, 40), -wm * sn(d1 * b, 40));
        const u32 gs = pk(ws * cs(d1 * kk, 40), -ws * sn(d1 * kk, 40));
        t.v[T_GS0 + i][l][j] = g < 2 ? gs : 0; t.v[T_GS1 + i][l][j] = g < 2 ? 0 : gs;
      }
    }
    const int d = 32 + 2 * g;
    const u32 mr = pk(cs(d * n, 40), cs((d + 1) * n, 40)), mi = pk(-sn(d * n, 40), -sn((d + 1) * n, 40));
    const u32 sr = pk(cs(d * ks, 40), cs((d + 1) * ks, 40)), si = pk(-sn(d * ks, 40), -sn((d + 1) * ks, 40));
    t.v[T_K16R][l][0] = mr; t.v[T_K16R][l][3] = mr; t.v[T_K16I][l][0] = mi; t.v[T_K16I][l][3] = mi;
    t.v[T_K16S][l][0] = lo ? sr : 0; t.v[T_K16S][l][1] = lo ? 0 : sr; t.v[T_K16S][l][2] = lo ? si : 0; t.v[T_K16S][l][3] = lo ? 0 : si;
  }
  return t;
}
static __device__ const Tab TAB = make_tab();

DI u32 cvtpk(float lo, float hi) { f32x2 v = {lo, hi}; bf16x2 b = __builtin_convertvector(v, bf16x2); return __builtin_bit_cast(u32, b); }
DI float bflo(u32 u) { return __builtin_bit_cast(float, u << 16); }
DI float bfhi(u32 u) { return __builtin_bit_cast(float, u & 0xffff0000u); }
DI float rbf(float x) { return bflo(cvtpk(x, x)); }
DI u32 nosent(u32 v) { return v < 0xfffffffeu ? v : 0xfffffffeu; }
DI u32 max3u(u32 a, u32 b, u32 c) { u32 m = a > b ? a : b; return m > c ? m : c; }
DI bool any_marker(u32 m) { return __builtin_amdgcn_ballot_w64(m == 0xffffffffu) != 0; }

DI u32x4 ld4(rsrc_t r, u32 off, u32 so = 0) { return __builtin_amdgcn_raw_buffer_load_b128(r, off, so, 0); }
DI u32x2 ld2(rsrc_t r, u32 off, u32 so = 0) { return __builtin_amdgcn_raw_buffer_load_b64(r, off, so, 0); }
DI u32x4 ld4c(rsrc_t r, u32 off, u32 so = 0) { return __builtin_amdgcn_raw_buffer_load_b128(r, off, so, SC1); }
DI u32x3 ld3c(rsrc_t r, u32 off, u32 so = 0) { return __builtin_amdgcn_raw_buffer_load_b96(r, off, so, SC1); }
DI u32x2 ld2c(rsrc_t r, u32 off, u32 so = 0) { return __builtin_amdgcn_raw_buffer_load_b64(r, off, so, SC1); }
DI u32 ld1c(rsrc_t r, u32 off, u32 so = 0) { return __builtin_amdgcn_raw_buffer_load_b32(r, off, so, SC1); }
DI void st4(rsrc_t r, u32 off, u32 so, u32x4 v) { __builtin_amdgcn_raw_buffer_store_b128(v, r, off, so, SC1); }
DI void st2(rsrc_t r, u32 off, u32 so, u32x2 v) { __builtin_amdgcn_raw_buffer_store_b64(v, r, off, so, SC1); }
DI void st1(rsrc_t r, u32 off, u32 so, u32 v) { __builtin_amdgcn_raw_buffer_store_b32(v, r, off, so, SC1); }
template <class T> DI T lds_ld(u32 a) { return *(const LAS T*)(size_t)a; }
template <class T> DI void lds_st(u32 a, T v) { *(LAS T*)(size_t)a = v; }
#define WAIT_VM(n) asm volatile("s_waitcnt vmcnt(" #n ")" ::: "memory")
#define FENCE() asm volatile("" ::: "memory")
#define PIN(x) asm volatile("" : "+v"(x))
#define PINS(x) asm volatile("" : "+s"(x))
DI void bar() { asm volatile("s_waitcnt lgkmcnt(0)\n\ts_barrier" ::: "memory"); }

DI void swap32u(u32& a, u32& b) { auto r = __builtin_amdgcn_permlane32_swap(a, b, false, false); a = r[0]; b = r[1]; }
DI void swap16u(u32& a, u32& b) { auto r = __builtin_amdgcn_permlane16_swap(a, b, false, false); a = r[0]; b = r[1]; }
DI void swap32(float& a, float& b) { u32 x = __builtin_bit_cast(u32, a), y = __builtin_bit_cast(u32, b); swap32u(x, y); a = __builtin_bit_cast(float, x); b = __builtin_bit_cast(float, y); }
DI void swap16(float& a, float& b) { u32 x = __builtin_bit_cast(u32, a), y = __builtin_bit_cast(u32, b); swap16u(x, y); a = __builtin_bit_cast(float, x); b = __builtin_bit_cast(float, y); }
#define ROR(n) "s_nop 1\n\tv_add_f32_dpp %0, %0, %0 row_ror:" #n " row_mask:0xf bank_mask:0xf\n\t"
DI float row_sum(float v) { asm(ROR(8) ROR(4) ROR(2) ROR(1) "s_nop 0" : "+v"(v)); return v; }
#define ROR2(n) "v_add_f32_dpp %0, %0, %0 row_ror:" #n " row_mask:0xf bank_mask:0xf\n\tv_add_f32_dpp %1, %1, %1 row_ror:" #n " row_mask:0xf bank_mask:0xf\n\ts_nop 0\n\t"
DI void row_sum2(float& a, float& b) { asm("s_nop 1\n\t" ROR2(8) ROR2(4) ROR2(2) ROR2(1) : "+v"(a), "+v"(b)); }
DI void wave_sum2(float a, float b, float& sa, float& sb) {
  swap32(a, b); float v = a + b, u = v; swap16(v, u); v = row_sum(v + u);
  sa = __builtin_bit_cast(float, __builtin_amdgcn_readlane(__builtin_bit_cast(int, v), 0));
  sb = __builtin_bit_cast(float, __builtin_amdgcn_readlane(__builtin_bit_cast(int, v), 32));
}

#define M32 "v_mfma_f32_16x16x32_bf16 "
#define M16 "v_mfma_f32_16x16x16_bf16 "
template <bool DA> DI void mm3(f32x4 (&re)[3], f32x4 (&im)[3], const u32x4 (&d)[3], u32x4 fr, u32x4 fi) {
  if (DA)
    asm volatile("s_nop 1\n\t" M32 "%0, %6, %9, 0\n\t" M32 "%3, %6, %10, 0\n\t" M32 "%1, %7, %9, 0\n\t" M32 "%4, %7, %10, 0\n\t" M32 "%2, %8, %9, 0\n\t" M32 "%5, %8, %10, 0\n\ts_nop 7"
                 : "=&v"(re[0]), "=&v"(re[1]), "=&v"(re[2]), "=&v"(im[0]), "=&v"(im[1]), "=&v"(im[2]) : "v"(d[0]), "v"(d[1]), "v"(d[2]), "v"(fr), "v"(fi));
  else
    asm volatile("s_nop 1\n\t" M32 "%0, %9, %6, 0\n\t" M32 "%3, %10, %6, 0\n\t" M32 "%1, %9, %7, 0\n\t" M32 "%4, %10, %7, 0\n\t" M32 "%2, %9, %8, 0\n\t" M32 "%5, %10, %8, 0\n\ts_nop 7"
                 : "=&v"(re[0]), "=&v"(re[1]), "=&v"(re[2]), "=&v"(im[0]), "=&v"(im[1]), "=&v"(im[2]) : "v"(d[0]), "v"(d[1]), "v"(d[2]), "v"(fr), "v"(fi));
}
DI void mm6k16(f32x4 (&o)[6], u32x2 m, const u32x2 (&d)[6]) {
  asm volatile("s_nop 1\n\t" M16 "%0, %6, %7, 0\n\t" M16 "%1, %6, %8, 0\n\t" M16 "%2, %6, %9, 0\n\t" M16 "%3, %6, %10, 0\n\t" M16 "%4, %6, %11, 0\n\t" M16 "%5, %6, %12, 0\n\ts_nop 7"
               : "=&v"(o[0]), "=&v"(o[1]), "=&v"(o[2]), "=&v"(o[3]), "=&v"(o[4]), "=&v"(o[5])
               : "v"(m), "v"(d[0]), "v"(d[1]), "v"(d[2]), "v"(d[3]), "v"(d[4]), "v"(d[5]));
}
template <int SG> DI u32x4 cmul_pack(f32x4 re, f32x4 im, f32x4 c, f32x4 s) {
  f32x4 a, b;
  if (SG > 0) { a = re * c - im * s; b = im * c + re * s; } else { a = re * c + im * s; b = im * c - re * s; }
  return u32x4{cvtpk(a[0], b[0]), cvtpk(a[1], b[1]), cvtpk(a[2], b[2]), cvtpk(a[3], b[3])};
}

#define ACLOB "a0","a1","a2","a3","a4","a5","a6","a7","a8","a9","a10","a11","a12","a13","a14","a15","a16","a17","a18","a19","a20","a21","a22","a23","a24","a25","a26","a27","a28","a29","a30","a31","a32","a33","a34","a35","a36","a37","a38","a39","a40","a41","a42","a43","a44","a45","a46","a47","a48","a49","a50","a51","a52","a53","a54","a55","a56","a57","a58","a59","a60","a61","a62","a63","a64","a65","a66","a67","a68","a69","a70","a71","a72","a73","a74","a75","a76","a77","a78","a79","a80","a81","a82","a83","a84","a85","a86","a87","a88","a89","a90","a91","a92","a93","a94","a95","a96","a97","a98","a99","a100","a101","a102","a103","a104","a105","a106","a107","a108","a109","a110","a111","a112","a113","a114","a115","a116","a117","a118","a119","a120","a121","a122","a123","a124","a125","a126","a127","a128","a129","a130","a131","a132","a133","a134","a135","a136","a137","a138","a139","a140","a141","a142","a143","a144","a145","a146","a147","a148","a149","a150","a151","a152","a153","a154","a155","a156","a157","a158","a159","a160","a161","a162","a163","a164","a165","a166","a167","a168","a169","a170","a171","a172","a173","a174","a175","a176","a177","a178","a179","a180","a181","a182","a183","a184","a185","a186","a187","a188","a189","a190","a191","a192","a193","a194","a195","a196","a197","a198","a199","a200","a201","a202","a203","a204","a205","a206","a207","a208","a209","a210","a211","a212","a213","a214","a215","a216","a217","a218","a219","a220","a221","a222","a223","a224","a225","a226","a227","a228","a229","a230","a231","a232","a233","a234","a235","a236","a237","a238","a239"
template <int Q, int IMM> DI void load_a(u32 voff, rsrc_t rs, u32 soff) {
  asm volatile("buffer_load_dwordx4 a[%c3:%c4], %0, %1, %2 offen offset:%c5 sc1" ::"v"(voff), "s"(rs), "s"(soff), "i"(4 * Q), "i"(4 * Q + 3), "i"(IMM) : "memory", ACLOB);
}
DI u32 rot10(u32 c, u32 rot) { const u32 x = c + rot; return x >= 10 ? x - 10 : x; }
template <int C, int F0 = 0, int F1 = 6> DI void load_w1(u32 voff, rsrc_t rs, u32 base, u32 rot) {
  const u32 s0 = base + rot10(C, rot) * 6144, s1 = s0 + 4096;
  if (F0 <= 0 && 0 < F1) load_a<6 * C + 0, 0>(voff, rs, s0);
  if (F0 <= 1 && 1 < F1) load_a<6 * C + 1, 1024>(voff, rs, s0);
  if (F0 <= 2 && 2 < F1) load_a<6 * C + 2, 2048>(voff, rs, s0);
  if (F0 <= 3 && 3 < F1) load_a<6 * C + 3, 3072>(voff, rs, s0);
  if (F0 <= 4 && 4 < F1) load_a<6 * C + 4, 0>(voff, rs, s1);
  if (F0 <= 5 && 5 < F1) load_a<6 * C + 5, 1024>(voff, rs, s1);
}
DI void dma2(u32 voff, rsrc_t rs, u32 soff, u32 ldsa) {
  asm volatile("s_mov_b32 m0, %3\n\ts_nop 0\n\tbuffer_load_dwordx4 %0, %1, %2 offen sc1 lds\n\tbuffer_load_dwordx4 %0, %1, %2 offen offset:1024 sc1 lds"
               ::"v"(voff), "s"(rs), "s"(soff), "s"(ldsa) : "memory");
}
DI void dma1(u32 voff, rsrc_t rs, u32 soff, u32 ldsa) {
  asm volatile("s_mov_b32 m0, %3\n\ts_nop 0\n\tbuffer_load_dwordx4 %0, %1, %2 offen lds" ::"v"(voff), "s"(rs), "s"(soff), "s"(ldsa) : "memory");
}

struct GS {
  f32x4 acc[2][4][3], ac2[4][5]; u32x4 fr[8], vr[4], ha[4][6], wd[15]; u32x4 xa, xb; u32x2 xc;
  u32 ad[2][3], va[3], pkd[4], vm, wv, hv[2], xo, xo2, ho, ra[2], fail, rot, dbase, lbase, w1base;
  u32 w2s[4], hs[4];
  rsrc_t rs;
};
#define HB0 64
constexpr int EP0 = 82;
enum { O_NONE = 0, O_DMA = 1000, O_W1 = 2000, O_WV = 3000, O_WA = 4000, O_HA = 5000, O_HB = 6000, O_X1 = 7000 };
constexpr int P_W1[12] = {5, 6, 10, 11, 12, 13, 14, 15, 21, 22, 26, 27};
constexpr int P_WV[15] = {28, 29, 30, 31, 37, 38, 42, 43, 44, 45, 46, 47, 53, 54, 58};
constexpr int g1_op(int n) {
  if ((n & 15) < 2 && (n >> 4) + 3 < 10) return O_DMA + 2 * ((n >> 4) + 3) + (n & 1);
  for (int i = 0; i < 12; ++i) if (P_W1[i] == n) return O_W1 + 48 + i;
  for (int i = 0; i < 15; ++i) if (P_WV[i] == n) return O_WV + i;
  if (n >= 84 && n <= 156 && !(n & 1)) return O_WA + (n - 84) / 2 - (n > 96 ? 1 : 0);
  if (n >= 137 && (n & 1)) return O_HA + (n - 137) / 2;
  return O_NONE;
}
constexpr int n_st(int n) { return (n >= EP0 && n < EP0 + 27 && (n - EP0) % 9 == 8) ? 1 : 0; }
constexpr int g1_cnt(int n) { return (g1_op(n) == O_NONE ? 0 : g1_op(n) < O_W1 ? 2 : 1) + n_st(n); }
constexpr int g1_range(int a, int b) { int c = 0; for (int m = a; m < b; ++m) c += g1_cnt(m); return c; }
constexpr int g1_pos(int op) { for (int m = 0; m < 160; ++m) if (g1_op(m) == op) return m; return -1; }
constexpr int NWA1 = 36;
static_assert(g1_pos(O_WA + NWA1 - 1) > 0 && g1_pos(O_WA + NWA1) < 0 && g1_pos(O_HA + 11) == 159, "schedule");
constexpr bool wa_ok() { for (int f = 0; f < NWA1; ++f) if (g1_pos(O_WA + f) < 84 + 4 * (f / 3)) return false; return true; }
static_assert(wa_ok(), "a down-projection fragment would overwrite gate/up weights still in use");
constexpr int cmin(int a, int b) { return a < b ? a : b; }
template <int F> DI void load_wa(GS& s) { load_a<F, (F % 15 % 4) * 1024>(s.wv, s.rs, s.w2s[1 + F / 15] + (F % 15 / 4) * 4096); }
template <int F> DI void load_wv(GS& s) {
  asm volatile("buffer_load_dwordx4 %0, %1, %2, %3 offen offset:%c4 sc1" : "=v"(s.wd[F]) : "v"(s.wv), "s"(s.rs), "s"(s.w2s[0] + (F / 4) * 4096), "i"((F % 4) * 1024) : "memory");
}
template <int P, int F, int B> DI void h_load1(GS& s) {
  if constexpr (B == 0) asm volatile("buffer_load_dwordx4 %0, %1, %2, %3 offen offset:%c4 sc1" : "=v"(s.ha[P][F]) : "v"(s.hv[F & 1]), "s"(s.rs), "s"(s.hs[P]), "i"((F >> 1) * 1024) : "memory");
  else asm volatile("buffer_load_dwordx4 %0, %1, %2, %3 offen offset:%c4 sc1" : "+v"(s.ha[P][F]) : "v"(s.hv[F & 1]), "s"(s.rs), "s"(s.hs[P] + 12288), "i"((F >> 1) * 1024) : "memory");
}
template <int K, int H> DI void g1_dma(GS& s) {
  constexpr int P = K / 5, C = 2 * (K % 5) + H;
  dma2(s.wv, s.rs, s.dbase + P * 8192 + rot10(C, s.rot) * 16384, s.lbase + P * 8192 + C * 16384);
}
template <int OP> DI void do_op(GS& s) {
  if constexpr (OP >= O_X1) {
    if constexpr (OP == O_X1) asm volatile("buffer_load_dwordx4 %0, %1, %2, 0 offen sc1" : "=v"(s.xa) : "v"(s.xo), "s"(s.rs) : "memory");
    if constexpr (OP == O_X1 + 1) asm volatile("buffer_load_dwordx4 %0, %1, %2, 0 offen offset:64 sc1" : "=v"(s.xb) : "v"(s.xo), "s"(s.rs) : "memory");
    if constexpr (OP == O_X1 + 2) asm volatile("buffer_load_dwordx2 %0, %1, %2, 0 offen offset:128 sc1" : "=v"(s.xc) : "v"(s.xo2), "s"(s.rs) : "memory");
  } else if constexpr (OP >= O_HB) h_load1<(OP - O_HB) / 6, (OP - O_HB) % 6, 1>(s);
  else if constexpr (OP >= O_HA) h_load1<(OP - O_HA) / 6, (OP - O_HA) % 6, 0>(s);
  else if constexpr (OP >= O_WA) load_wa<OP - O_WA>(s);
  else if constexpr (OP >= O_WV) load_wv<OP - O_WV>(s);
  else if constexpr (OP >= O_W1) load_w1<(OP - O_W1) / 6, (OP - O_W1) % 6, (OP - O_W1) % 6 + 1>(s.wv, s.rs, s.w1base, s.rot);
  else if constexpr (OP >= O_DMA) g1_dma<(OP - O_DMA) / 2, (OP - O_DMA) & 1>(s);
}
template <int N, int I> DI void g1_mfma(GS& s) {
  constexpr int P = N / 80, KS = (N % 80) / 4, T = N & 3, q = 12 * KS + 4 * I;
  if constexpr (KS == 0) asm volatile(M32 "%0, a[%c2:%c3], %1, 0" : "=&v"(s.acc[P][T][I]) : "v"(s.fr[N & 7]), "i"(q), "i"(q + 3));
  else asm volatile(M32 "%0, a[%c2:%c3], %1, %0" : "+v"(s.acc[P][T][I]) : "v"(s.fr[N & 7]), "i"(q), "i"(q + 3));
}
template <int N> DI void g1_read(GS& s) {
  constexpr int P = N / 80, KS = (N % 80) / 4, T = N & 3, C = KS >> 1;
  asm volatile("ds_read_b128 %0, %1 offset:%c2" : "=v"(s.fr[N & 7]) : "v"(s.ad[KS & 1][C >> 2]), "i"((C & 3) * 16384 + P * 8192 + T * 2048) : "memory");
}
template <int K, int J> DI void g1_vread(GS& s) {
  constexpr int P = K / 5, C = 2 * (K % 5) + (J >> 1);
  asm volatile("ds_read_b128 %0, %1 offset:%c2" : "=v"(s.vr[J]) : "v"(s.va[C >> 2]), "i"((C & 3) * 16384 + P * 8192 + (J & 1) * 1024) : "memory");
}
template <bool FIRST> DI void vmax4(u32& m, u32x4 v) {
  if constexpr (FIRST) asm volatile("v_max3_u32 %0, %1, %2, %3\n\tv_max_u32 %0, %0, %4" : "=&v"(m) : "v"(v[0]), "v"(v[1]), "v"(v[2]), "v"(v[3]));
  else asm volatile("v_max3_u32 %0, %0, %1, %2\n\tv_max3_u32 %0, %0, %3, %4" : "+v"(m) : "v"(v[0]), "v"(v[1]), "v"(v[2]), "v"(v[3]));
}
template <int K> DI void g1_check(GS& s) {
  if (__builtin_expect(any_marker(s.vm), 0)) {
    for (u32 sp = 0;; ++sp) {
      if (sp > SPIN_CAP || s.fail) { s.fail = 1; break; }
      g1_dma<K, 0>(s); g1_dma<K, 1>(s); WAIT_VM(0);
      g1_vread<K, 0>(s); g1_vread<K, 1>(s); g1_vread<K, 2>(s); g1_vread<K, 3>(s);
      asm volatile("s_waitcnt lgkmcnt(0)" ::: "memory");
      vmax4<true>(s.vm, s.vr[0]); vmax4<false>(s.vm, s.vr[1]); vmax4<false>(s.vm, s.vr[2]); vmax4<false>(s.vm, s.vr[3]);
      if (!any_marker(s.vm)) break;
    }
  }
  asm volatile("s_barrier" ::: "memory");
}
template <int P, int I, int J, int H> DI void g1_epi(GS& s) {
  if constexpr (J < 4) {
    f32x4& a = s.acc[P][J][I];
    if constexpr (H == 0) asm volatile("v_max_f32 %0, 0, %0\n\tv_max_f32 %1, 0, %1" : "+v"(a[0]), "+v"(a[2]));
    else asm volatile("v_mul_f32 %0, %0, %2\n\tv_mul_f32 %1, %1, %3" : "+v"(a[0]), "+v"(a[2]) : "v"(a[1]), "v"(a[3]));
  } else if constexpr (J < 6) {
    constexpr int T = 2 * (J - 4) + H;
    asm volatile("v_cvt_pk_bf16_f32 %0, %1, %2\n\tv_min_u32 %0, -2, %0" : "=&v"(s.pkd[T]) : "v"(s.acc[P][T][I][0]), "v"(s.acc[P][T][I][2]));
  } else if constexpr (J == 6) {
    asm volatile("s_nop 1\n\tv_permlane32_swap_b32 %0, %1" : "+v"(s.pkd[H]), "+v"(s.pkd[H + 2]));
  } else if constexpr (J == 7) {
    asm volatile("s_nop 1\n\tv_permlane16_swap_b32 %0, %1" : "+v"(s.pkd[2 * H]), "+v"(s.pkd[2 * H + 1]));
  } else if constexpr (H == 1) {
    st4(s.rs, s.ho + I * 256, P * 12288, u32x4{s.pkd[0], s.pkd[1], s.pkd[2], s.pkd[3]});
  }
}
template <int N> DI void g1_step(GS& s) {
  constexpr int ph = N & 15, K = (N >> 4) + 1;
  constexpr bool val = K < 10;
  if constexpr (val && ph == 9) g1_check<K>(s);
  g1_mfma<N, 0>(s);
  if constexpr (N + 7 < 160) g1_read<N + 7>(s);
  g1_mfma<N, 1>(s);
  if constexpr (val && ph == 2)
    asm volatile("s_waitcnt vmcnt(%c0)" ::"i"(cmin(63, K < 3 ? 4 * (2 - K) + g1_range(0, N) : g1_range(g1_pos(O_DMA + 2 * K + 1) + 1, N))) : "memory");
  if constexpr (N == 62) asm volatile("s_waitcnt vmcnt(%c0)" ::"i"(g1_range(g1_pos(O_W1 + 59) + 1, N)) : "memory");
  do_op<g1_op(N)>(s);
  if constexpr (val && ph == 3) g1_vread<K, 1>(s);
  if constexpr (val && ph == 4) g1_vread<K, 3>(s);
  if constexpr (val && ph == 7) vmax4<true>(s.vm, s.vr[0]);
  if constexpr (val && ph == 8) vmax4<false>(s.vm, s.vr[2]);
  if constexpr (N >= EP0 && N < EP0 + 27) g1_epi<0, (N - EP0) / 9, (N - EP0) % 9, 0>(s);
  g1_mfma<N, 2>(s);
  if constexpr (val && ph == 2) g1_vread<K, 0>(s);
  if constexpr (val && ph == 3) g1_vread<K, 2>(s);
  if constexpr (val && ph == 7) vmax4<false>(s.vm, s.vr[1]);
  if constexpr (val && ph == 8) vmax4<false>(s.vm, s.vr[3]);
  if constexpr (N >= EP0 && N < EP0 + 27) g1_epi<0, (N - EP0) / 9, (N - EP0) % 9, 1>(s);
  if constexpr (val && ph == 6) asm volatile("s_waitcnt lgkmcnt(2)" ::: "memory");
  else if constexpr ((N & 1) && N < 159) asm volatile("s_waitcnt lgkmcnt(%c0)" ::"i"(N + 7 < 160 ? 5 : (N + 3 < 160 ? 157 - N : 0)) : "memory");
}
template <int... N> DI void g1_all(GS& s, std::integer_sequence<int, N...>) { (g1_step<N>(s), ...); }
DI void g1_epi_b(GS& s) {
#pragma unroll
  for (int i = 0; i < 3; ++i) {
    u32 p[4];
#pragma unroll
    for (int t = 0; t < 4; ++t) {
      const f32x4 a = s.acc[1][t][i];
      float g0, g1;
      asm("v_max_f32 %0, 0, %1" : "=v"(g0) : "v"(a[0])); asm("v_max_f32 %0, 0, %1" : "=v"(g1) : "v"(a[2]));
      asm("v_mul_f32 %0, %0, %1" : "+v"(g0) : "v"(a[1])); asm("v_mul_f32 %0, %0, %1" : "+v"(g1) : "v"(a[3]));
      p[t] = nosent(cvtpk(g0, g1));
    }
    swap32u(p[0], p[2]); swap32u(p[1], p[3]); swap16u(p[0], p[1]); swap16u(p[2], p[3]);
    st4(s.rs, s.ho + i * 256, 12288, u32x4{p[0], p[1], p[2], p[3]});
  }
}

constexpr int NPRE = 9;
constexpr int g2_op(int m) {
  if (m & 1) return O_NONE;
  if (m >= 2 && m < 2 + 2 * (45 - NWA1)) return O_WA + NWA1 + (m - 2) / 2;
  if (m >= 22 && m < 34) return O_HA + 18 + (m - 22) / 2;
  if (m >= HB0 && m < HB0 + 24) return O_HB + (m - HB0) / 2;
  if (m >= 92 && m < 104) return O_HB + 12 + (m - 92) / 2;
  if (m >= 122 && m < 134) return O_HB + 18 + (m - 122) / 2;
  if (m >= 136 && m < 142) return O_X1 + (m - 136) / 2;
  return O_NONE;
}
constexpr int g2_range(int a, int b) { int c = 0; for (int m = a; m < b; ++m) c += g2_op(m) != O_NONE; return c; }
constexpr int g2_pos(int op) { for (int m = 0; m < 240; ++m) if (g2_op(m) == op) return m; return -1; }
constexpr int g2_wait(int c, int m) {
  if (c == 0) return g1_range(g1_pos(O_HA + 5) + 1, 160) + NPRE + g2_range(0, m);
  if (c == 1) return NPRE + g2_range(0, m);
  if (c == 2) return g2_range(0, m);
  return g2_range(g2_pos((c < 4 ? O_HA : O_HB - 24) + 6 * c + 5) + 1, m);
}
static_assert(HB0 >= 48 && HB0 + 24 <= 92 && g1_pos(O_WA + 29) < g1_pos(O_HA + 11) && g1_pos(O_WA + 14) < g1_pos(O_HA + 5), "schedule");
#define H6(c, h) c(h[0]), c(h[1]), c(h[2]), c(h[3]), c(h[4]), c(h[5])
template <int C> DI void g2_check(GS& s) {
  constexpr int P = C & 3, B = C >> 2;
  if (__builtin_expect(any_marker(s.vm), 0)) {
    for (u32 sp = 0;; ++sp) {
      if (sp > SPIN_CAP || s.fail) { s.fail = 1; break; }
      const u32 so = s.hs[P] + B * 12288;
#define HL(i, v, o) "buffer_load_dwordx4 %" #i ", %" #v ", %8, %9 offen offset:" #o " sc1\n\t"
      asm volatile(HL(0, 6, 0) HL(1, 7, 0) HL(2, 6, 1024) HL(3, 7, 1024) HL(4, 6, 2048) HL(5, 7, 2048) "s_waitcnt vmcnt(0)"
                   : H6("+v", s.ha[P]) : "v"(s.hv[0]), "v"(s.hv[1]), "s"(s.rs), "s"(so) : "memory");
      vmax4<true>(s.vm, s.ha[P][0]); vmax4<false>(s.vm, s.ha[P][1]); vmax4<false>(s.vm, s.ha[P][2]);
      vmax4<false>(s.vm, s.ha[P][3]); vmax4<false>(s.vm, s.ha[P][4]); vmax4<false>(s.vm, s.ha[P][5]);
      if (!any_marker(s.vm)) break;
    }
  }
}
template <int M> DI void g2_step(GS& s) {
  constexpr int C = M / 30, P = C & 3, B = C >> 2, F = (M % 30) / 5, I = M % 5, KK = F >> 1, R = M % 30;
  if constexpr (R == 0 && C > 0) g2_check<C>(s);
  if constexpr (M == 120) asm volatile("s_barrier" ::: "memory");
  f32x4& acc = s.ac2[2 * B + (F & 1)][I];
  if constexpr (P == 0) {
    if constexpr (KK == 0) asm volatile(M32 "%0, %1, %2, 0" : "=&v"(acc) : "v"(s.wd[5 * KK + I]), "v"(s.ha[P][F]));
    else asm volatile(M32 "%0, %1, %2, %0" : "+v"(acc) : "v"(s.wd[5 * KK + I]), "v"(s.ha[P][F]));
  } else {
    constexpr int q = 4 * (15 * (P - 1) + 5 * KK + I);
    asm volatile(M32 "%0, a[%c2:%c3], %1, %0" : "+v"(acc) : "v"(s.ha[P][F]), "i"(q), "i"(q + 3));
  }
  do_op<g2_op(M)>(s);
  if constexpr (C < 7) {
    if constexpr (R == 21) asm volatile("s_waitcnt vmcnt(%c0)" ::"i"(cmin(63, g2_wait(C + 1, M + 1))) : "memory");
    if constexpr (R >= 22 && R < 28) vmax4<R == 22>(s.vm, s.ha[(C + 1) & 3][R - 22]);
  }
  if constexpr (M >= 145 && M < 205 && (M - 145) % 6 == 0) {
    constexpr int J = (M - 145) / 6;
    asm volatile("ds_write_b128 %0, %1 offset:%c2" ::"v"(s.ra[J / 5]), "v"(s.ac2[J / 5][J % 5]), "i"((J % 5) * 1024) : "memory");
  }
}
template <int... M> DI void g2_all(GS& s, std::integer_sequence<int, M...>) { (g2_step<M>(s), ...); }

DI u32x2 tr_read(u32 a) { u32x2 r; asm volatile("ds_read_b64_tr_b16 %0, %1" : "=v"(r) : "v"(a) : "memory"); return r; }

template <int R> DI void strip_fma(f32x2 (&a)[4], const u32x4 (&wv)[R], const float (&sc)[R]) {
#pragma unroll
  for (int j = 0; j < 4; ++j) a[j] = f32x2{0, 0};
#pragma unroll
  for (int r = 0; r < R; ++r)
#pragma unroll
    for (int j = 0; j < 4; ++j) a[j] += sc[r] * f32x2{bflo(wv[r][j]), bfhi(wv[r][j])};
}
DI void strip_fold(const f32x2 (&a)[4], float& u, float& v) {
  float s[4];
#pragma unroll
  for (int j = 0; j < 4; ++j) { float x = a[j >> 1][j & 1], y = a[2 + (j >> 1)][j & 1]; swap32(x, y); s[j] = x + y; }
  swap16(s[0], s[2]); swap16(s[1], s[3]);
  u = s[0] + s[2]; v = s[1] + s[3];
}
DI void row_sum2q(float& a, float& b) { asm("s_nop 1\n\t" ROR2(8) ROR2(4) : "+v"(a), "+v"(b)); }
DI u32 mod_pack(float a, float b, bool scale) { const u32 v = cvtpk(a, b); return scale ? cvtpk(1.f + bflo(v), 1.f + bfhi(v)) : v; }
DI float silu_bf(u32 cb) { const float c = bflo(cb); return rbf(c * __builtin_amdgcn_rcpf(1.f + __expf(-c))); }
DI void ln_stats(const f32x2 (&v)[10], f32x2& rs, f32x2& ms) {
  f32x2 s = v[0];
#pragma unroll
  for (int i = 1; i < 10; ++i) s += v[i];
  float m0, m1; wave_sum2(s[0], s[1], m0, m1);
  const f32x2 m = f32x2{m0, m1} * (1.f / 640);
  f32x2 q = {0, 0};
#pragma unroll
  for (int i = 0; i < 10; ++i) { const f32x2 a = v[i] - m; q += a * a; }
  float t0, t1; wave_sum2(q[0], q[1], t0, t1);
  u32 p = cvtpk(t0 * (1.f / 640), t1 * (1.f / 640));
  p = cvtpk(bflo(p) + 1e-5f, bfhi(p) + 1e-5f);
  p = cvtpk(__builtin_amdgcn_sqrtf(bflo(p)), __builtin_amdgcn_sqrtf(bfhi(p)));
  rs = f32x2{__builtin_amdgcn_rcpf(bflo(p)), __builtin_amdgcn_rcpf(bfhi(p))};
  p = cvtpk(m[0], m[1]);
  ms = -f32x2{bflo(p), bfhi(p)} * rs;
}
DI u32 mod_addr(u32 gp) {
  const u32 vec = gp / 160, d = 4 * (gp - vec * 160);
  return vec < 2 ? L_MSC + (d + 16 * (d >> 7)) * 8 + (vec == 0 ? 4 : 0) : (L_M3 + d * 16 + (vec == 2 ? 0 : vec == 4 ? 4 : vec == 3 ? 8 : 12)) | 0x80000000u;
}
DI void mod_scatter(u32 a0, u32x4 g) {
  const u32 a = a0 & 0x7fffffffu, st = 8 + ((a0 >> 31) << 3);
  lds_st<float>(a, bflo(g[0])); lds_st<float>(a + st, bfhi(g[0])); lds_st<float>(a + 2 * st, bflo(g[2])); lds_st<float>(a + 3 * st, bfhi(g[2]));
}
#define STAMP(i) do {} while (0)
#define DLY0 2
#define DLYS 4
#define DLY1 1
#define DLY2 8
#define DLY3 8
#define TABQ(T, q) lds_ld<T>(L_TAB + (q) * 1024 + wv)

__global__ void __launch_bounds__(256, 1) fused_kernel(const void* px, const void* pwr, const void* pwi, const void* pwg, const void* pwu, const void* pwd,
                                                       const void* pc, const void* pwm, void* pout, void* pws) {
  STAMP(0);
  const u32 tid = threadIdx.x & 255, lane = tid & 63, l15 = lane & 15, g = lane >> 4, wv = lane * 16;
  const u32 w = __builtin_amdgcn_readfirstlane(tid >> 6);
  const u32 b = blockIdx.x, xcd = b & 7, rr = b >> 3, pi = 32 * xcd + rr;
  const u32 mt = 4 * (xcd >> 1) + (rr >> 3), nt = 8 * (xcd & 1) + (rr & 7), half = xcd & 1, ct = rr & 7;
  const rsrc_t rC = __builtin_amdgcn_make_buffer_rsrc((void*)pc, 0, 640 * 2, 0x00020000);
  const rsrc_t rWm = __builtin_amdgcn_make_buffer_rsrc((void*)pwm, 0, 640 * 3840 * 2, 0x00020000);
  const rsrc_t rX = __builtin_amdgcn_make_buffer_rsrc((void*)px, 0, 2048 * 640 * 2, 0x00020000);
  const rsrc_t rS = __builtin_amdgcn_make_buffer_rsrc(pws, 0, WS_USED, 0x00020000);
  u32 fail = 0;

  const u32 t2 = tid & 127, w2 = w & 1;
  const bool crit = rr < 20, rest = rr >= 20 && rr < 30;
  const u32x4 ones = {~0u, ~0u, ~0u, ~0u};
  u32x4 wa[20], xr[3]; u32 cv[20];
  const rsrc_t rT = __builtin_amdgcn_make_buffer_rsrc((void*)&TAB, 0, T_N * 1024, 0x00020000);
  const u32 sA = 20 * xcd + rr, sB = 10 * xcd + rr - 20;
  if (w == 0 && crit) {
    const u32 r0 = lane + 80 * (sA & 7);
#pragma unroll
    for (int j = 0; j < 10; ++j) { u32 k = r0 + 64 * j; k = k >= 640 ? k - 640 : k; wa[j] = ld4(rWm, k * 7680, sA * 16); cv[j] = __builtin_amdgcn_raw_buffer_load_b16(rC, k * 2, 0, 0); }
  } else if (w < 2 && rest) {
    const u32 k0 = 320 * (w2 ^ (sB & 1)) + (lane >> 2);
#pragma unroll
    for (int j = 0; j < 20; ++j) { wa[j] = ld4(rWm, k0 * 7680 + (lane & 3) * 16, 2560 + sB * 64 + j * 16 * 7680); cv[j] = __builtin_amdgcn_raw_buffer_load_b16(rC, k0 * 2 + j * 32, 0, 0); }
  } else if (w >= 2) {
    __builtin_amdgcn_s_sleep(DLYS);
#pragma unroll
    for (int j = 0; j < 8; ++j) dma1(wv, rT, (w2 + 2 * j) * 1024, L_TAB + (w2 + 2 * j) * 1024);
  }
  FENCE();
  const u32 tagv = ld1c(rS, OFF_EPOCH);
  {
    const u32 so = (pi + 512 * w) * 1280;
    xr[0] = ld4(rX, wv, so);
    xr[1] = ld4(rX, lane < 16 ? wv + 1024 : wv + 256 * 1280 - 256, so);
    xr[2] = ld4(rX, lane < 32 ? wv + 256 * 1280 + 768 : OOB, so);
  }
  FENCE();
  u32x4 pg[5], pu[5], pd[6], fr4[2], fi4[2]; u32x2 fr2[2], fi2[2];
  if (w >= 2) {
    const rsrc_t rWr = __builtin_amdgcn_make_buffer_rsrc((void*)pwr, 0, 2048 * 321 * 2, 0x00020000);
    const rsrc_t rWi = __builtin_amdgcn_make_buffer_rsrc((void*)pwi, 0, 2048 * 321 * 2, 0x00020000);
    const rsrc_t rWg = __builtin_amdgcn_make_buffer_rsrc((void*)pwg, 0, 640 * 1536 * 2, 0x00020000);
    const rsrc_t rWu = __builtin_amdgcn_make_buffer_rsrc((void*)pwu, 0, 640 * 1536 * 2, 0x00020000);
    const rsrc_t rWd = __builtin_amdgcn_make_buffer_rsrc((void*)pwd, 0, 640 * 1536 * 2, 0x00020000);
    const u32 o1 = (lane & 7) < 6 ? (lane >> 3) * 16 * 3072 + (lane & 7) * 16 : OOB;
#pragma unroll
    for (int j = 0; j < 5; ++j) {
      const u32 q = 5 * mt + j, d0 = q < 64 ? 128 * (q >> 4) + (q & 15) : 512 + (q - 64);
      const u32 so = d0 * 3072 + nt * 192 + w2 * 96;
      pg[j] = ld4(rWg, o1, so); pu[j] = ld4(rWu, o1, so);
    }
    const u32 o2 = l15 < 10 ? g * 1280 + l15 * 16 : OOB;
#pragma unroll
    for (int j = 0; j < 6; ++j) pd[j] = ld4(rWd, o2, (48 * (2 * mt + half) + 24 * w2 + 4 * j) * 1280 + ct * 160);
#pragma unroll
    for (int j = 0; j < 2; ++j) {
      const u32 fo = (((xcd + 8 * (rr >> 1) + 128 * (8 * (rr & 1) + 4 * j + g)) * 321 + 10 * (16 * w2 + l15)) & ~1u) * 2;
      fr4[j] = ld4(rWr, fo); fr2[j] = ld2(rWr, fo + 16); fi4[j] = ld4(rWi, fo); fi2[j] = ld2(rWi, fo + 16);
    }
  } else if (w == 1) {
#pragma unroll
    for (int j = 0; j < 11; ++j) st4(rS, wv + (j & 3) * 1024, OFF_E1 + pi * E1P + (j >> 2) * 4096, ones);
#pragma unroll
    for (int j = 0; j < 12; ++j) st4(rS, wv + (j & 3) * 1024, OFF_E2 + pi * E2P + (j >> 2) * 4096, ones);
  } else {
    unsigned long long pcv; u32 len, junk;
    asm volatile("s_getpc_b64 %0\n.Lk_start:\n\ts_mov_b32 %1, .Lk_end-.Lk_start" : "=s"(pcv), "=s"(len));
    const u32 o = (lane + 64 * (rr & 7)) * 128;
    asm volatile("global_load_dword %0, %1, %2" : "=v"(junk) : "v"(o < len ? o : 0), "s"(pcv) : "memory");
  }
  FENCE();
  STAMP(1);
  u32x2 m8f, m8i;
#pragma unroll
  for (int e = 0; e < 2; ++e) {
    const float a = (float)(((l15 >> 1) * (256 * (2 * g + e) + pi)) & 2047) * (1.f / 2048), c = __builtin_amdgcn_cosf(a), s = __builtin_amdgcn_sinf(a);
    m8f[e] = (l15 & 1) ? cvtpk(-s, c) : cvtpk(c, s);
    const float a2 = (float)(((2 * g + e) * (256 * (l15 >> 1) + pi)) & 2047) * (1.f / 2048), c2 = __builtin_amdgcn_cosf(a2), s2 = __builtin_amdgcn_sinf(a2);
    m8i[e] = (l15 & 1) ? cvtpk(s2, c2) : cvtpk(c2, -s2);
  }
  PIN(m8f); PIN(m8i);
  u32 aMain, aShrR, aShrW;
  {
    const u32 cmain = g < 2 ? l15 + 160 * g : 40 - l15 + 40 * (12 - 4 * g);
    const u32 k1s = 16 + (l15 & 7), k1c = k1s > 20 ? 20 : k1s, cshr = g < 2 ? k1c + 160 * g : 40 - k1c + 40 * (12 - 4 * g);
    aMain = L_S3 + w * S3P + cmain * 8; aShrR = L_S3 + w * S3P + cshr * 8 + (l15 >> 3) * 4;
    aShrW = k1s > 20 ? L_S3 + 4 * S3P + lane * 4 : aShrR;
  }
  PIN(aMain); PIN(aShrR); PIN(aShrW);
  u32 tag;
  if (w == 0 && crit) {
    float sc[10]; u32x4 wq[10]; f32x2 a[4]; float u, v;
#pragma unroll
    for (int j = 0; j < 10; ++j) { sc[j] = silu_bf(cv[j]); wq[j] = wa[j]; }
    strip_fma<10>(a, wq, sc); strip_fold(a, u, v); row_sum2(u, v);
    tag = __builtin_amdgcn_readfirstlane(tagv) + 1;
    st2(rS, l15 == 0 ? g * 8 : OOB, OFF_MODG + sA * 32, u32x2{mod_pack(u, v, sA >= 80), tag});
  } else if (w < 2 && rest) {
    float sc[20]; f32x2 a[4]; float u, v;
#pragma unroll
    for (int j = 0; j < 20; ++j) sc[j] = silu_bf(cv[j]);
    strip_fma<20>(a, wa, sc); strip_fold(a, u, v); row_sum2q(u, v);
    if (l15 < 4) lds_st<f32x2>(L_RED + (w * 16 + l15 * 4 + g) * 8, f32x2{u, v});
  }
  tag = __builtin_amdgcn_readfirstlane(tagv) + 1;
  STAMP(2);
  lds_st<u32x4>(L_XS + w * 2560 + wv, xr[0]); lds_st<u32x4>(L_XS + w * 2560 + wv + 1024, xr[1]);
  if (lane < 32) lds_st<u32x4>(L_XS + w * 2560 + wv + 2048, xr[2]);
  f32x2 xv[10], nv[10];
  const u32 xsa = L_XS + w * 2560 + g * 256 + l15 * 2, xsb = L_XS + w * 2560 + 1024 + g * 64 + l15 * 2;
#pragma unroll
  for (int i = 0; i < 10; ++i) {
    const u32 a = i < 8 ? xsa + i * 32 : xsb + (i - 8) * 32;
    xv[i] = f32x2{bflo(lds_ld<unsigned short>(a)), bflo(lds_ld<unsigned short>(a + 1280))};
  }
  {
    f32x2 rs, ms; ln_stats(xv, rs, ms);
#pragma unroll
    for (int i = 0; i < 10; ++i) { nv[i] = xv[i] * rs + ms; PIN(nv[i]); PIN(xv[i]); }
  }
  STAMP(3);
  bar();
  STAMP(4);
  u32x4 c1[15];
#pragma unroll
  for (int q = 0; q < 15; ++q) { c1[q] = TABQ(u32x4, q); PIN(c1[q]); }
  if (w >= 2) {
    const u32 p1 = L_P1 + w2 * 40 * P1P, p2 = L_P2 + w2 * 24 * P2P;
    if ((lane & 7) < 6) {
#pragma unroll
      for (int j = 0; j < 5; ++j) {
        u32x4 lo, hi;
        lo[0] = __builtin_amdgcn_perm(pu[j][0], pg[j][0], 0x05040100); lo[1] = __builtin_amdgcn_perm(pu[j][0], pg[j][0], 0x07060302);
        lo[2] = __builtin_amdgcn_perm(pu[j][1], pg[j][1], 0x05040100); lo[3] = __builtin_amdgcn_perm(pu[j][1], pg[j][1], 0x07060302);
        hi[0] = __builtin_amdgcn_perm(pu[j][2], pg[j][2], 0x05040100); hi[1] = __builtin_amdgcn_perm(pu[j][2], pg[j][2], 0x07060302);
        hi[2] = __builtin_amdgcn_perm(pu[j][3], pg[j][3], 0x05040100); hi[3] = __builtin_amdgcn_perm(pu[j][3], pg[j][3], 0x07060302);
        const u32 a = p1 + ((lane >> 3) + 8 * j) * P1P + (lane & 7) * 32;
        lds_st<u32x4>(a, lo); lds_st<u32x4>(a + 16, hi);
      }
    }
    if (l15 < 10) {
#pragma unroll
      for (int j = 0; j < 6; ++j) lds_st<u32x4>(p2 + (g + 4 * j) * P2P + l15 * 16, pd[j]);
    }
    const u32 fs = L_FS + w2 * 6400;
#pragma unroll
    for (int k = 0; k < 2; ++k) {
      const u32 sh = (xcd & 1) * 16;
      const u32 r[6] = {fr4[k][0], fr4[k][1], fr4[k][2], fr4[k][3], fr2[k][0], fr2[k][1]}, im[6] = {fi4[k][0], fi4[k][1], fi4[k][2], fi4[k][3], fi2[k][0], fi2[k][1]};
      const u32 a = fs + l15 * 400 + (4 * k + g) * 4;
#pragma unroll
      for (int j = 0; j < 6; ++j) {
        const u32 rj = __builtin_amdgcn_alignbit(j < 5 ? r[j + 1] : 0u, r[j], sh), ij = __builtin_amdgcn_alignbit(j < 5 ? im[j + 1] : 0u, im[j], sh);
        lds_st<u32>(a + j * 64, __builtin_amdgcn_perm(ij, rj, 0x05040100));
        lds_st<u32>(a + j * 64 + 32, __builtin_amdgcn_perm(ij, rj, 0x07060302));
      }
    }
    asm volatile("s_waitcnt lgkmcnt(0)" ::: "memory");
#pragma unroll
    for (int j = 0; j < 9; ++j) dma1(wv, rT, (16 + w2 + 2 * j) * 1024, L_TAB + (16 + w2 + 2 * j) * 1024);
#pragma unroll
    for (int j = 0; j < 6; ++j) {
      const u32 idx = lane + 64 * j, cg = (idx * 683) >> 14, of = idx - 24 * cg;
      st4(rS, cg * FWP + of * 16, OFF_FW + (32 * xcd + 16 * w2) * FWP + rr * 400, lds_ld<u32x4>(fs + cg * 400 + of * 16));
    }
    const u32 q4 = l15 >> 2, p = l15 & 3;
    u32x2 t1[12][2]; u32 oo[12];
#pragma unroll
    for (int j = 0; j < 8; ++j) {
      const u32 ci = j < 5 ? j : j < 7 ? g : 4, tl = j < 5 ? g : j < 7 ? j - 1 : 4 + (g & 1), ti = 6 * w2 + tl, wq = 2 * w2 + (tl >= 3), q = 5 * mt + ci;
      const u32 a = p1 + (8 * ci + q4) * P1P + tl * 32 + p * 8;
      t1[j][0] = tr_read(a); t1[j][1] = tr_read(a + 4 * P1P);
      oo[j] = (j == 7 && g >= 2) ? OOB : OFF_WG1 + ((nt * 4 + wq) * 60 + (q >> 2) * 3 + (ti - 3 * wq)) * 1024 + ((q & 3) * 16 + l15) * 16;
    }
#pragma unroll
    for (int j = 0; j < 4; ++j) {
      const u32 ci = j < 3 ? j : (g < 3 ? g : 2), ti = j < 3 ? g : 4, Q = 6 * (2 * mt + half) + 3 * w2 + ci, KS = Q >> 2, kq = mt >> 2;
      const u32 a = p2 + (8 * ci + q4) * P2P + (j < 3 ? (32 * (g >> 1) + 8 * p + 4 * (g & 1)) * 2 : (64 + 4 * p) * 2);
      t1[8 + j][0] = tr_read(a); t1[8 + j][1] = tr_read(a + 4 * P2P);
      oo[8 + j] = (j == 3 && g == 3) ? OOB : OFF_WG2 + ((ct * 4 + kq) * 60 + (KS - kq * 12) * 5 + ti) * 1024 + ((Q & 3) * 16 + l15) * 16;
    }
#define T2(j) "+v"(t1[j][0]), "+v"(t1[j][1])
    asm volatile("s_waitcnt lgkmcnt(0)" : T2(0), T2(1), T2(2), T2(3), T2(4), T2(5), T2(6), T2(7), T2(8), T2(9), T2(10), T2(11) :: "memory");
#pragma unroll
    for (int j = 0; j < 12; ++j) st4(rS, oo[j], 0, u32x4{t1[j][0][0], t1[j][0][1], t1[j][1][0], t1[j][1][1]});
    STAMP(5);
  } else {
    if (rest && tid < 16) {
      const f32x2 p = lds_ld<f32x2>(L_RED + tid * 8) + lds_ld<f32x2>(L_RED + 128 + tid * 8);
      st2(rS, tid * 8, OFF_MODG + 5120 + sB * 128, u32x2{mod_pack(p[0], p[1], sB >= 40 && sB < 60), tag});
    }
    if (w == 1) { WAIT_VM(0); if (lane == 0) st1(rS, OFF_FLG + b * 16, 0, tag); }
    u32 ma[3];
#pragma unroll
    for (int j = 0; j < 3; ++j) { ma[j] = mod_addr(t2 + 128 * j); PIN(ma[j]); }
    u32 o2 = t2 < 64 ? t2 * 16 + 4096 : t2 * 16; PIN(o2);
    STAMP(5);
    u32x4 gr[3];
    for (u32 sp = 0;; ++sp) {
      FENCE();
      gr[0] = ld4c(rS, t2 * 16, OFF_MODG); gr[1] = ld4c(rS, t2 * 16 + 2048, OFF_MODG); gr[2] = ld4c(rS, o2, OFF_MODG);
      u32 m = 0;
#pragma unroll
      for (int j = 0; j < 3; ++j) m |= (gr[j][1] ^ tag) | (gr[j][3] ^ tag);
      if (__builtin_amdgcn_ballot_w64(m != 0) == 0) break;
      if (sp > SPIN_CAP) { fail = 1; break; }
      __builtin_amdgcn_s_sleep(DLY0);
    }
    mod_scatter(ma[0], gr[0]); mod_scatter(ma[1], gr[1]);
    if (w == 0) mod_scatter(ma[2], gr[2]);
  }
  STAMP(6);
  bar();
  STAMP(7);
  {
    u32x4 a32[2]; u32x2 a16;
    {
      f32x2 h[10];
      const u32 a0 = L_MSC + (144 * g + l15) * 8, a1 = L_MSC + (576 + 32 * g + l15) * 8;
#pragma unroll
      for (int i = 0; i < 10; ++i) {
        const f32x2 m = lds_ld<f32x2>(i < 8 ? a0 + i * 128 : a1 + (i - 8) * 128);
        h[i] = nv[i] * m[0] + m[1];
      }
#pragma unroll
      for (int e = 0; e < 2; ++e) {
        a32[e] = u32x4{cvtpk(h[0][e], h[1][e]), cvtpk(h[2][e], h[3][e]), cvtpk(h[4][e], h[5][e]), cvtpk(h[6][e], h[7][e])};
        a16[e] = cvtpk(h[8][e], h[9][e]);
      }
    }
    f32x4 cr[3], ci[3];
    {
      const u32x2 r0 = {c1[T_K16R][0], c1[T_K16R][1]}, r1 = {c1[T_K16R][2], c1[T_K16R][3]}, i0 = {c1[T_K16I][0], c1[T_K16I][1]}, i1 = {c1[T_K16I][2], c1[T_K16I][3]};
      const u32x2 sr = {c1[T_K16S][0], c1[T_K16S][1]}, si = {c1[T_K16S][2], c1[T_K16S][3]};
      asm volatile("s_nop 1\n\t"
                   M32 "%0, %6, %9, 0\n\t" M32 "%3, %6, %10, 0\n\t" M32 "%1, %7, %9, 0\n\t" M32 "%4, %7, %10, 0\n\t"
                   M32 "%2, %6, %11, 0\n\t" M32 "%5, %6, %13, 0\n\t"
                   M16 "%0, %8, %15, %0\n\t" M16 "%3, %8, %17, %3\n\t" M16 "%1, %8, %16, %1\n\t" M16 "%4, %8, %18, %4\n\t"
                   M32 "%2, %7, %12, %2\n\t" M32 "%5, %7, %14, %5\n\t" M16 "%2, %8, %19, %2\n\t" M16 "%5, %8, %20, %5\n\ts_nop 7"
                   : "=&v"(cr[0]), "=&v"(cr[1]), "=&v"(cr[2]), "=&v"(ci[0]), "=&v"(ci[1]), "=&v"(ci[2])
                   : "v"(a32[0]), "v"(a32[1]), "v"(a16), "v"(c1[T_BM32R]), "v"(c1[T_BM32I]), "v"(c1[T_S0R]), "v"(c1[T_S1R]), "v"(c1[T_S0I]), "v"(c1[T_S1I]),
                     "v"(r0), "v"(r1), "v"(i0), "v"(i1), "v"(sr), "v"(si));
    }
#define F4(x) __builtin_bit_cast(f32x4, x)
    u32x4 dd[3] = {cmul_pack<-1>(cr[0], ci[0], F4(c1[T_TWC]), F4(c1[T_TWS])), cmul_pack<-1>(cr[1], ci[1], F4(c1[T_TWC]), F4(c1[T_TWS])),
                   cmul_pack<-1>(cr[2], ci[2], F4(c1[T_TSC]), F4(c1[T_TSS]))};
    mm3<false>(cr, ci, dd, c1[T_K1R], c1[T_K1I]);
#pragma unroll
    for (int r = 0; r < 4; ++r) {
      lds_st<u32x2>(aMain + r * 320, u32x2{cvtpk(cr[0][r], ci[0][r]), cvtpk(cr[1][r], ci[1][r])});
      lds_st<u32>(aShrW + r * 320, cvtpk(cr[2][r], ci[2][r]));
    }
    if (w >= 2) WAIT_VM(18);
    bar();
    u32x2 d8[6]; f32x4 o8[6];
    {
      const u32 a = L_S3 + g * S3P + w * 640;
      const u32x4 v0 = lds_ld<u32x4>(a + l15 * 32), v1 = lds_ld<u32x4>(a + l15 * 32 + 16);
      d8[0] = u32x2{v0[0], v0[1]}; d8[1] = u32x2{v0[2], v0[3]}; d8[2] = u32x2{v1[0], v1[1]}; d8[3] = u32x2{v1[2], v1[3]};
      d8[4] = lds_ld<u32x2>(a + 512 + l15 * 8); d8[5] = lds_ld<u32x2>(a + 640 + l15 * 8);
    }
    mm6k16(o8, m8f, d8);
    const u32 o = g * 2 * E1R + w * 320, so = OFF_E1 + pi * E1P, o5 = (w == 3 && l15 < 2) ? o + 320 + l15 * 4 : OOB;
#pragma unroll
    for (int k = 0; k < 2; ++k) {
      u32 v[6];
#pragma unroll
      for (int t = 0; t < 6; ++t) v[t] = nosent(cvtpk(o8[t][2 * k], o8[t][2 * k + 1]));
      st4(rS, o + l15 * 16 + k * E1R, so, u32x4{v[0], v[1], v[2], v[3]});
      st1(rS, o + l15 * 4 + 256 + k * E1R, so, v[4]);
      st1(rS, o5 + k * E1R, so, v[5]);
    }
  }
  STAMP(8);
  const u32 rot = ((nt & 7) + 3 * (mt & 3)) % 10, rot2 = ct & 3;
  const u32 w1base = OFF_WG1 + (nt * 4 + w) * 61440;

  {
    u32x4 c2[6];
#pragma unroll
    for (int q = 0; q < 6; ++q) { c2[q] = TABQ(u32x4, T_T2C + q); PIN(c2[q]); }
    u32 ua = (3 * w) * KIP + l15 * 80 + g * 16, ao = L_KO + (64 * g + l15) * 48 + w * 12;
    u32 uf = L_WL + (2 * l15 + (g >> 1)) * 400 + w * 96 + (g & 1) * 16;
    PIN(ua); PIN(ao); PIN(uf);
    if (w < 2) {
      u32 go = (16 * ((lane >> 2) & 3) + g + 4 * w) * E1P + (lane & 3) * 12, lo = L_KI + (lane & 3) * 3 * KIP + ((g + 4 * w) * 20 + ((lane >> 2) & 3)) * 4;
      PIN(go); PIN(lo);
      const u32 gso = OFF_E1 + xcd * E1R + rr * 40;
      for (u32 sp = 0;; ++sp) {
        FENCE();
        const u32 f0 = ld1c(rS, wv, OFF_FLG), f1 = ld1c(rS, wv + 1024, OFF_FLG), f2 = ld1c(rS, wv + 2048, OFF_FLG), f3 = ld1c(rS, wv + 3072, OFF_FLG);
        if (__builtin_amdgcn_ballot_w64(((f0 ^ tag) | (f1 ^ tag) | (f2 ^ tag) | (f3 ^ tag)) != 0) == 0) break;
        if (sp > SPIN_CAP || fail) { fail = 1; break; }
      }
      if (b == 0 && tid == 0) st1(rS, OFF_EPOCH, 0, tag);
      __builtin_amdgcn_s_sleep(DLY1);
      STAMP(9);
      u32x3 v[8];
      for (u32 sp = 0;; ++sp) {
        FENCE();
#pragma unroll
        for (int j = 0; j < 8; ++j) v[j] = ld3c(rS, go, gso + ((j & 3) * 64 + (j >> 2) * 8) * E1P);
        u32 m = 0;
#pragma unroll
        for (int j = 0; j < 8; j += 2) { m = max3u(m, v[j][0], v[j][1]); m = max3u(m, v[j][2], v[j + 1][0]); m = max3u(m, v[j + 1][1], v[j + 1][2]); }
        if (!any_marker(m)) break;
        if (sp > SPIN_CAP || fail) { fail = 1; break; }
      }
      STAMP(10);
#pragma unroll
      for (int j = 0; j < 8; ++j) {
        const u32 a = lo + (j & 3) * 16 + (j >> 2) * 640;
        lds_st<u32>(a, v[j][0]); lds_st<u32>(a + KIP, v[j][1]); lds_st<u32>(a + 2 * KIP, v[j][2]);
      }
    } else {
      WAIT_VM(6);
      if (lane == 0) st1(rS, OFF_FLG + b * 16 + 8 + w2 * 4, 0, tag);
#pragma unroll
      for (int j = 0; j < 5; ++j) st4(rS, wv + (2 * j + w2) * 16384, OFF_H2 + mt * 163840 + nt * 1024, ones);
      const u32 fo = (lane < 32 ? ((lane >> 2) & 7) + 8 * (8 * (lane & 3) + ct) : xcd + 8 * (lane - 32)) * 16 + 8;
      for (u32 sp = 0;; ++sp) {
        FENCE();
        const u32x2 f = ld2c(rS, fo, OFF_FLG);
        if (__builtin_amdgcn_ballot_w64(((f[0] ^ tag) | (f[1] ^ tag)) != 0) == 0) break;
        if (sp > SPIN_CAP || fail) { fail = 1; break; }
      }
      STAMP(9);
      dma2(wv, rS, OFF_FW + pi * FWP + w2 * 6144, L_WL + w2 * 6144); dma2(wv, rS, OFF_FW + pi * FWP + w2 * 6144 + 2048, L_WL + w2 * 6144 + 2048);
      dma2(wv, rS, OFF_FW + pi * FWP + w2 * 6144 + 4096, L_WL + w2 * 6144 + 4096);
      if (w2) dma2(wv, rS, OFF_FW + pi * FWP + 11264, L_WL + 11264); else dma2(wv, rS, OFF_FW + pi * FWP, L_WL);
      load_w1<0>(wv, rS, w1base, rot); load_w1<1>(wv, rS, w1base, rot); load_w1<2>(wv, rS, w1base, rot);
      load_w1<3>(wv, rS, w1base, rot); load_w1<4>(wv, rS, w1base, rot); load_w1<5>(wv, rS, w1base, rot);
      WAIT_VM(36);
      STAMP(10);
    }
    bar();
    STAMP(11);
    u32x4 dd[3], wf[3]; f32x4 cr[3], ci[3], wfr[3], wfi[3];
#pragma unroll
    for (int j = 0; j < 3; ++j) dd[j] = lds_ld<u32x4>(L_KI + ua + j * KIP);
#pragma unroll
    for (int j = 0; j < 3; ++j) wf[j] = lds_ld<u32x4>(uf + j * 32);
    mm3<true>(cr, ci, dd, c2[2], c2[3]);
    if (w < 2) load_w1<0, 0, 2>(wv, rS, w1base, rot); else load_w1<6, 0, 2>(wv, rS, w1base, rot);
#pragma unroll
    for (int j = 0; j < 3; ++j) dd[j] = cmul_pack<-1>(cr[j], ci[j], F4(c2[0]), F4(c2[1]));
    mm3<false>(cr, ci, dd, c2[2], c2[3]);
    if (w < 2) load_w1<0, 2, 4>(wv, rS, w1base, rot); else load_w1<6, 2, 4>(wv, rS, w1base, rot);
#pragma unroll
    for (int j = 0; j < 3; ++j) {
#pragma unroll
      for (int r = 0; r < 4; ++r) { wfr[j][r] = bflo(wf[j][r]); wfi[j][r] = bfhi(wf[j][r]); }
      dd[j] = cmul_pack<1>(cr[j], ci[j], wfr[j], wfi[j]);
    }
    mm3<true>(cr, ci, dd, c2[4], c2[5]);
    if (w < 2) load_w1<0, 4, 6>(wv, rS, w1base, rot); else load_w1<6, 4, 6>(wv, rS, w1base, rot);
#pragma unroll
    for (int j = 0; j < 3; ++j) dd[j] = cmul_pack<1>(cr[j], ci[j], F4(c2[0]), F4(c2[1]));
    mm3<false>(cr, ci, dd, c2[4], c2[5]);
#pragma unroll
    for (int r = 0; r < 4; ++r)
#pragma unroll
      for (int j = 0; j < 3; ++j) lds_st<u32>(ao + r * 768 + j * 4, nosent(cvtpk(cr[j][r], ci[j][r])));
    bar();
    u32x4 ov[3];
#pragma unroll
    for (int j = 0; j < 3; ++j) ov[j] = lds_ld<u32x4>(L_KO + tid * 16 + j * 4096);
#pragma unroll
    for (int j = 0; j < 3; ++j) st4(rS, tid * 16, OFF_E2 + pi * E2P + j * 4096, ov[j]);
  }
  STAMP(12);

  u32 gbp[10];
  {
    u32x4 c3[13];
#pragma unroll
    for (int q = 0; q < 13; ++q) { c3[q] = TABQ(u32x4, T_IAR + q); PIN(c3[q]); }
    u32x4 twc = TABQ(u32x4, T_TWC), tws = TABQ(u32x4, T_TWS); PIN(twc); PIN(tws);
    if (w >= 2) {
      u32 go[3], lw[3], lx[3];
#pragma unroll
      for (int i = 0; i < 3; ++i) {
        const u32 sg = 16 * i + l15, pc = (sg * 43) >> 7, ch = sg - 3 * pc;
        go[i] = (64 * g + 16 * w2 + pc) * E2P + ch * 16;
        lw[i] = L_S3 + g * S3P + (160 * w2 + 10 * pc + 4 * ch) * 8;
        lx[i] = (ch < 2 || (w2 == 1 && pc == 15)) ? lw[i] + 16 : L_S3 + 4 * S3P + wv;
        PIN(go[i]); PIN(lw[i]); PIN(lx[i]);
      }
      const u32 gso = OFF_E2 + 48 * pi;
      __builtin_amdgcn_s_sleep(DLY2);
      STAMP(13);
      u32x4 v[3][2];
      for (u32 sp = 0;; ++sp) {
        FENCE();
#pragma unroll
        for (int i = 0; i < 3; ++i) { v[i][0] = ld4c(rS, go[i], gso); v[i][1] = ld4c(rS, go[i], gso + 32 * E2P); }
        u32 m = 0;
#pragma unroll
        for (int i = 0; i < 6; ++i) { m = max3u(m, v[i >> 1][i & 1][0], v[i >> 1][i & 1][1]); m = max3u(m, v[i >> 1][i & 1][2], v[i >> 1][i & 1][3]); }
        if (!any_marker(m)) break;
        if (sp > SPIN_CAP || fail) { fail = 1; break; }
      }
      STAMP(14);
#pragma unroll
      for (int hh = 0; hh < 2; ++hh) {
        u32x2 d8[6]; f32x4 o8[6];
#pragma unroll
        for (int i = 0; i < 3; ++i) { d8[2 * i] = u32x2{v[i][0][2 * hh], v[i][1][2 * hh]}; d8[2 * i + 1] = u32x2{v[i][0][2 * hh + 1], v[i][1][2 * hh + 1]}; }
        mm6k16(o8, m8i, d8);
#pragma unroll
        for (int i = 0; i < 3; ++i)
          lds_st<u32x4>(hh ? lx[i] : lw[i], u32x4{cvtpk(o8[2 * i][0], o8[2 * i][1]), cvtpk(o8[2 * i][2], o8[2 * i][3]), cvtpk(o8[2 * i + 1][0], o8[2 * i + 1][1]), cvtpk(o8[2 * i + 1][2], o8[2 * i + 1][3])});
      }
    } else {
#pragma unroll
      for (int j = 0; j < 12; ++j) st4(rS, t2 * 16 + (j & 1) * 2048, OFF_H + (mt * 16 + nt) * 24576 + (j >> 1) * 4096, ones);
      u32x4 gr[5];
      for (u32 sp = 0;; ++sp) {
        FENCE();
#pragma unroll
        for (int j = 0; j < 5; ++j) gr[j] = ld4c(rS, t2 * 16 + (j & 1) * 2048, OFF_MODG + 5120 + (j >> 1) * 4096);
        u32 m = 0;
#pragma unroll
        for (int j = 0; j < 5; ++j) m |= (gr[j][1] ^ tag) | (gr[j][3] ^ tag);
        if (__builtin_amdgcn_ballot_w64(m != 0) == 0) break;
        if (sp > SPIN_CAP || fail) { fail = 1; break; }
      }
      STAMP(13);
      load_w1<1>(wv, rS, w1base, rot); load_w1<2>(wv, rS, w1base, rot); load_w1<3>(wv, rS, w1base, rot);
#pragma unroll
      for (int j = 0; j < 5; ++j) mod_scatter(mod_addr(320 + t2 + 128 * j), gr[j]);
      load_w1<4>(wv, rS, w1base, rot); load_w1<5>(wv, rS, w1base, rot); load_w1<6>(wv, rS, w1base, rot);
      STAMP(14);
    }
    bar();
    u32x4 dd[3]; f32x4 cr[3], ci[3];
#pragma unroll
    for (int j = 0; j < 4; ++j) {
      const u32x2 t = lds_ld<u32x2>(aMain + j * 320);
      dd[0][j] = t[0]; dd[1][j] = t[1]; dd[2][j] = lds_ld<u32>(aShrR + j * 320);
    }
    FENCE();
    f32x4 m3[10];
#pragma unroll
    for (int i = 0; i < 10; ++i) m3[i] = lds_ld<f32x4>(L_M3 + l15 * 16 + (i < 8 ? g * 2048 + i * 256 : 8192 + g * 512 + (i - 8) * 256));
    mm3<true>(cr, ci, dd, c3[0], c3[1]);
    load_w1<7, 0, 2>(wv, rS, w1base, rot);
    dd[0] = cmul_pack<1>(cr[0], ci[0], F4(twc), F4(tws)); dd[1] = cmul_pack<1>(cr[1], ci[1], F4(twc), F4(tws)); dd[2] = cmul_pack<1>(cr[2], ci[2], F4(c3[2]), F4(c3[3]));
    f32x4 y[2][3];
    asm volatile("s_nop 1\n\t"
                 M32 "%0, %9, %6, 0\n\t" M32 "%3, %9, %7, 0\n\t" M32 "%1, %10, %6, 0\n\t" M32 "%4, %10, %7, 0\n\t" M32 "%2, %11, %6, 0\n\t" M32 "%5, %11, %7, 0\n\t"
                 M32 "%0, %12, %8, %0\n\t" M32 "%3, %15, %8, %3\n\t" M32 "%1, %13, %8, %1\n\t" M32 "%4, %16, %8, %4\n\t" M32 "%2, %14, %8, %2\n\t" M32 "%5, %17, %8, %5\n\ts_nop 7"
                 : "=&v"(y[0][0]), "=&v"(y[0][1]), "=&v"(y[0][2]), "=&v"(y[1][0]), "=&v"(y[1][1]), "=&v"(y[1][2])
                 : "v"(dd[0]), "v"(dd[1]), "v"(dd[2]), "v"(c3[4]), "v"(c3[5]), "v"(c3[6]), "v"(c3[7]), "v"(c3[8]), "v"(c3[9]), "v"(c3[10]), "v"(c3[11]), "v"(c3[12]));
    load_w1<7, 2, 4>(wv, rS, w1base, rot);
    f32x2 x1[10];
#pragma unroll
    for (int i = 0; i < 10; ++i) {
      const f32x2 t = xv[i] + (m3[i][0] * (1.f / (2048.f * 640.f))) * f32x2{y[0][i >> 2][i & 3], y[1][i >> 2][i & 3]};
      const u32 p = cvtpk(t[0], t[1]);
      x1[i] = f32x2{bflo(p), bfhi(p)};
      const u32 a = i < 8 ? xsa + i * 32 : xsb + (i - 8) * 32;
      lds_st<unsigned short>(a, (unsigned short)p); lds_st<unsigned short>(a + 1280, (unsigned short)(p >> 16));
    }
    load_w1<7, 4, 6>(wv, rS, w1base, rot);
    f32x2 rs, ms; ln_stats(x1, rs, ms);
    f32x2 h[10];
#pragma unroll
    for (int i = 0; i < 10; ++i) h[i] = (x1[i] * rs + ms) * m3[i][1] + m3[i][2];
    const u32 so = OFF_H2 + mt * 163840 + nt * 1024 + w * 256;
#pragma unroll
    for (int e = 0; e < 2; ++e) {
      const u32 tok = 2 * w + e;
      st4(rS, (lane >> 3) * 16384 + (((lane & 7) ^ tok) * 16) + e * 128, so,
          u32x4{nosent(cvtpk(h[0][e], h[1][e])), nosent(cvtpk(h[2][e], h[3][e])), nosent(cvtpk(h[4][e], h[5][e])), nosent(cvtpk(h[6][e], h[7][e]))});
      st1(rS, (8 + (l15 >> 3)) * 16384 + (((l15 & 7) ^ tok) * 16) + g * 4 + e * 128, so, nosent(cvtpk(h[8][e], h[9][e])));
    }
    STAMP(15);
#pragma unroll
    for (int j = 0; j < 3; ++j)
      st4(rS, j < 2 || lane < 32 ? wv + j * 1024 : OOB, OFF_X1 + (128 * mt + 8 * nt + 2 * w) * 1280, lds_ld<u32x4>(L_XS + w * 2560 + wv + j * 1024));
#pragma unroll
    for (int i = 0; i < 10; ++i) {
      const u32 a = L_M3 + (80 * ct + (i < 8 ? 32 * (i >> 2) + 8 * g + 2 * (i & 3) : 64 + 4 * g + 2 * (i - 8))) * 16 + 12;
      gbp[i] = cvtpk(lds_ld<float>(a), lds_ld<float>(a + 16)); PIN(gbp[i]);
    }
  }
  GS s;
  const u32 tl = 16 * (2 * half + (w & 1) + 4 * (w >> 1)) + l15;
  s.rs = rS; s.wv = wv; s.fail = fail; s.rot = rot; s.w1base = w1base;
  s.dbase = OFF_H2 + mt * 163840 + w * 2048; s.lbase = w * 2048;
#pragma unroll
  for (int j = 0; j < 3; ++j) {
    s.va[j] = j * 65536 + w * 2048 + wv; PIN(s.va[j]);
#pragma unroll
    for (int h = 0; h < 2; ++h) { s.ad[h][j] = j * 65536 + (l15 >> 3) * 1024 + (l15 & 7) * 128 + (((4 * h + g) ^ (l15 & 7)) * 16); PIN(s.ad[h][j]); }
  }
  s.xo = OFF_X1 + (128 * mt + tl) * 1280 + ct * 160 + g * 16; PIN(s.xo); s.xo2 = s.xo - g * 8; PIN(s.xo2);
  s.ho = OFF_H + (mt * 16 + nt) * 24576 + g * 3072 + 3 * w * 256 + l15 * 16; PIN(s.ho);
  s.hv[0] = wv + (w & 1) * 3072; s.hv[1] = wv + (~w & 1) * 3072; PIN(s.hv[0]); PIN(s.hv[1]);
  s.ra[0] = (w & 1) * 20480 + w * 5120 + wv; s.ra[1] = (~w & 1) * 20480 + w * 5120 + wv; PIN(s.ra[0]); PIN(s.ra[1]);
#pragma unroll
  for (int p = 0; p < 4; ++p) {
    s.w2s[p] = OFF_WG2 + (ct * 4 + w) * 61440 + ((p + rot2) & 3) * 15360; PINS(s.w2s[p]);
    s.hs[p] = OFF_H + (mt * 16 + 4 * w + ((p + rot2) & 3)) * 24576 + 2 * half * 3072; PINS(s.hs[p]);
  }
  bar();
  __builtin_amdgcn_s_sleep(DLY3);
  g1_dma<0, 0>(s); g1_dma<0, 1>(s); g1_dma<1, 0>(s); g1_dma<1, 1>(s); g1_dma<2, 0>(s); g1_dma<2, 1>(s);
  WAIT_VM(8);
  g1_vread<0, 0>(s); g1_vread<0, 1>(s); g1_vread<0, 2>(s); g1_vread<0, 3>(s);
  asm volatile("s_waitcnt lgkmcnt(0)" ::: "memory");
  vmax4<true>(s.vm, s.vr[0]); vmax4<false>(s.vm, s.vr[1]); vmax4<false>(s.vm, s.vr[2]); vmax4<false>(s.vm, s.vr[3]);
  g1_check<0>(s);
  STAMP(16);
  g1_read<0>(s); g1_read<1>(s); g1_read<2>(s); g1_read<3>(s); g1_read<4>(s); g1_read<5>(s); g1_read<6>(s);
  asm volatile("s_waitcnt lgkmcnt(5)" ::: "memory");
  g1_all(s, std::make_integer_sequence<int, 160>{});
  STAMP(17);
  asm volatile("s_nop 7" : "+v"(s.acc[1][0][0]), "+v"(s.acc[1][0][1]), "+v"(s.acc[1][0][2]), "+v"(s.acc[1][1][0]), "+v"(s.acc[1][1][1]), "+v"(s.acc[1][1][2]),
               "+v"(s.acc[1][2][0]), "+v"(s.acc[1][2][1]), "+v"(s.acc[1][2][2]), "+v"(s.acc[1][3][0]), "+v"(s.acc[1][3][1]), "+v"(s.acc[1][3][2]));
  g1_epi_b(s);
  do_op<O_HA + 12>(s); do_op<O_HA + 13>(s); do_op<O_HA + 14>(s); do_op<O_HA + 15>(s); do_op<O_HA + 16>(s); do_op<O_HA + 17>(s);
  asm volatile("s_waitcnt vmcnt(%c0)" ::"i"(g2_wait(0, 0)) : "memory");
  vmax4<true>(s.vm, s.ha[0][0]); vmax4<false>(s.vm, s.ha[0][1]); vmax4<false>(s.vm, s.ha[0][2]);
  vmax4<false>(s.vm, s.ha[0][3]); vmax4<false>(s.vm, s.ha[0][4]); vmax4<false>(s.vm, s.ha[0][5]);
  g2_check<0>(s);
  g2_all(s, std::make_integer_sequence<int, 240>{});
  asm volatile("s_waitcnt vmcnt(0)\n\ts_nop 7" : "+v"(s.ac2[2][0]), "+v"(s.ac2[2][1]), "+v"(s.ac2[2][2]), "+v"(s.ac2[2][3]), "+v"(s.ac2[2][4]), "+v"(s.ac2[3][0]),
               "+v"(s.ac2[3][1]), "+v"(s.ac2[3][2]), "+v"(s.ac2[3][3]), "+v"(s.ac2[3][4]), "+v"(s.xa), "+v"(s.xb), "+v"(s.xc));
  STAMP(18);
#pragma unroll
  for (int i = 0; i < 5; ++i) lds_st<f32x4>(s.ra[1] + 40960 + i * 1024, s.ac2[3][i]);
  f32x4 fin[5];
  if (w < 2) {
#pragma unroll
    for (int i = 0; i < 5; ++i) lds_st<f32x4>(s.ra[0] + 40960 + i * 1024, s.ac2[2][i]);
#pragma unroll
    for (int i = 0; i < 5; ++i) fin[i] = lds_ld<f32x4>(w * 25600 + wv + i * 1024);
  } else {
#pragma unroll
    for (int i = 0; i < 5; ++i) fin[i] = s.ac2[2][i];
  }
  bar();
#pragma unroll
  for (int sl = 1; sl < 4; ++sl)
#pragma unroll
    for (int i = 0; i < 5; ++i) fin[i] += lds_ld<f32x4>(w * 20480 + ((w + sl) & 3) * 5120 + wv + i * 1024);
  {
    const u32 xs[10] = {s.xa[0], s.xa[1], s.xa[2], s.xa[3], s.xb[0], s.xb[1], s.xb[2], s.xb[3], s.xc[0], s.xc[1]};
    u32 o[10];
#pragma unroll
    for (int i = 0; i < 10; ++i) {
      const f32x2 v = f32x2{bflo(xs[i]), bfhi(xs[i])} + f32x2{bflo(gbp[i]), bfhi(gbp[i])} * f32x2{fin[i >> 1][2 * (i & 1)], fin[i >> 1][2 * (i & 1) + 1]};
      o[i] = cvtpk(v[0], v[1]);
    }
    if (s.fail) o[0] = 0x7fc07fc0u;
    const rsrc_t rO = __builtin_amdgcn_make_buffer_rsrc(pout, 0, 2048 * 640 * 2, 0x00020000);
    const u32 ntp = tl >> 3, row = 32 * (2 * (mt >> 2) + (ntp >> 3)) + 8 * (mt & 3) + (ntp & 7) + 256 * (tl & 7), oo = row * 1280 + ct * 160;
    __builtin_amdgcn_raw_buffer_store_b128(u32x4{o[0], o[1], o[2], o[3]}, rO, oo + g * 16, 0, 0);
    __builtin_amdgcn_raw_buffer_store_b128(u32x4{o[4], o[5], o[6], o[7]}, rO, oo + 64 + g * 16, 0, 0);
    __builtin_amdgcn_raw_buffer_store_b64(u32x2{o[8], o[9]}, rO, oo + 128 + g * 8, 0, 0);
  }
  asm volatile(".Lk_end:");
}

extern "C" void kernel_launch(void* const* d_in, const int* in_sizes, int n_in, void* d_out, int out_size, void* d_ws, size_t ws_size, hipStream_t stream) {
  (void)hipFuncSetAttribute((const void*)fused_kernel, hipFuncAttributeMaxDynamicSharedMemorySize, LDS_BYTES);
  fused_kernel<<<256, 256, LDS_BYTES, stream>>>(d_in[0], d_in[1], d_in[2], d_in[4], d_in[5], d_in[6], d_in[7], d_in[8], d_out, d_ws);
}
```

```cpp
#include <hip/hip_runtime.h>
#include <hip/hip_bf16.h>
#include <utility>

typedef unsigned u32;
typedef unsigned u32x2 __attribute__((ext_vector_type(2)));
typedef unsigned u32x3 __attribute__((ext_vector_type(3)));
typedef unsigned u32x4 __attribute__((ext_vector_type(4)));
typedef float f32x2 __attribute__((ext_vector_type(2)));
typedef float f32x4 __attribute__((ext_vector_type(4)));
typedef __bf16 bf16x2 __attribute__((ext_vector_type(2)));
typedef __amdgpu_buffer_rsrc_t rsrc_t;
#define DI __device__ __forceinline__
#define LAS __attribute__((address_space(3)))
#define SC1 16
#define NTS 0
#define NTF 0
#define NT 0

constexpr u32 OFF_EPOCH = 0, OFF_MODG = 0x1000, OFF_FLG = 0x5000, OFF_FA = 0x9000, OFF_FB = 0xA000;
constexpr u32 E1P = 11264, E1R = 1408, E2P = 12288;
constexpr u32 OFF_E1 = 0x10000, OFF_E2 = 0x2D0000, OFF_H2 = 0x5D0000, OFF_X1 = 0x850000, OFF_WG1 = 0xAD0000,
              OFF_WG2 = 0xE90000, OFF_H = 0x1070000, OFF_MODC = 0x1670000, WS_USED = 0x1680000, OFF_STAMP = 0x1F00000;
constexpr u32 OOB = 0x7ff00000u;
constexpr u32 L_TAB = 0, L_RED = 45056, L_MV = 45376, L_S3 = 61312, S3P = 2688, S4P = 1600, L_KI = 75648, KIP = 1328,
              L_WL = 91584, L_KO = 107520, L_P1 = 119808, P1P = 208, L_P2 = 136448, P2P = 176, L_SC = 144896, L_XS = 147456, XSP = 3072, LDS_BYTES = 163840;
constexpr u32 SPIN_CAP = 40000;

enum { T_BM32R, T_BM32I, T_S0R, T_S1R, T_S0I, T_S1I, T_K16R, T_K16I, T_K16S, T_TWC, T_TWS, T_TSC, T_TSS, T_K1R, T_K1I,
       T_T2C, T_T2S, T_F16R, T_F16I, T_I16R, T_I16I, T_IAR, T_IAI, T_USC, T_USS, T_GA, T_GS0 = T_GA + 3, T_GS1 = T_GS0 + 3, T_N = T_GS1 + 3 };
struct Trig { double c[1280]; };
constexpr double ccos(double x) { double t = 1, s = 1; for (int i = 1; i < 14; ++i) { t *= -x * x / ((2 * i - 1) * (2 * i)); s += t; } return s; }
constexpr Trig make_trig() {
  Trig r{};
  for (int j = 0; j <= 320; ++j) {
    const double v = j == 320 ? 0.0 : ccos(2 * 3.14159265358979323846 * j / 1280);
    r.c[j] = v; r.c[640 - j] = -v; r.c[640 + j] = -v; if (j) r.c[1280 - j] = v;
  }
  return r;
}
constexpr Trig TR = make_trig();
constexpr double cs(int p, int n) { return TR.c[(p % n) * (1280 / n)]; }
constexpr double sn(int p, int n) { return TR.c[((p % n) * (1280 / n) + 960) % 1280]; }
constexpr u32 fb(double v) { return __builtin_bit_cast(u32, (float)v); }
constexpr u32 bfr(double v) { const u32 u = fb(v + 0.0); return (u + 0x7fffu + ((u >> 16) & 1)) >> 16; }
constexpr u32 pk(double a, double b) { return bfr(a) | (bfr(b) << 16); }
struct Tab { u32 v[T_N][64][4]; };
constexpr Tab make_tab() {
  Tab t{};
  for (int l = 0; l < 64; ++l) {
    const int n = l & 15, g = l >> 4, ks = 16 + (n & 7); const bool lo = n < 8;
    for (int j = 0; j < 4; ++j) {
      const int d = 8 * g + 2 * j, b = 4 * g + j;
      t.v[T_BM32R][l][j] = pk(cs(d * n, 40), cs((d + 1) * n, 40)); t.v[T_BM32I][l][j] = pk(-sn(d * n, 40), -sn((d + 1) * n, 40));
      const u32 sr = pk(cs(d * ks, 40), cs((d + 1) * ks, 40)), si = pk(-sn(d * ks, 40), -sn((d + 1) * ks, 40));
      t.v[T_S0R][l][j] = lo ? sr : 0; t.v[T_S1R][l][j] = lo ? 0 : sr; t.v[T_S0I][l][j] = lo ? si : 0; t.v[T_S1I][l][j] = lo ? 0 : si;
      t.v[T_TWC][l][j] = fb(cs(b * n, 640)); t.v[T_TWS][l][j] = fb(sn(b * n, 640));
      t.v[T_TSC][l][j] = fb(cs(b * ks, 640)); t.v[T_TSS][l][j] = fb(sn(b * ks, 640));
      const int k2 = lo ? n : (n & 12) + 3 - (n & 3); const double sg = lo ? 1 : -1;
      t.v[T_K1R][l][j] = pk(cs(b * k2, 16), sn(b * k2, 16)); t.v[T_K1I][l][j] = pk(-sn(b * k2, 16) * sg, cs(b * k2, 16) * sg);
      t.v[T_T2C][l][j] = fb(cs(b * n, 256)); t.v[T_T2S][l][j] = fb(sn(b * n, 256));
      const double c = cs(b * n, 16), s = sn(b * n, 16);
      t.v[T_F16R][l][j] = pk(c, s); t.v[T_F16I][l][j] = pk(-s, c); t.v[T_I16R][l][j] = pk(c, -s); t.v[T_I16I][l][j] = pk(s, c);
      const int q2 = g < 2 ? b : 4 * g + 3 - j; const double s2 = g < 2 ? 1 : -1;
      t.v[T_IAR][l][j] = pk(cs(q2 * n, 16), -sn(q2 * n, 16) * s2); t.v[T_IAI][l][j] = pk(sn(q2 * n, 16), cs(q2 * n, 16) * s2);
      t.v[T_USC][l][j] = fb(cs((16 + (b & 7)) * n, 640)); t.v[T_USS][l][j] = fb(sn((16 + (b & 7)) * n, 640));
      for (int i = 0; i < 3; ++i) {
        const int d1 = i < 2 ? 8 * (n >> 2) + 4 * i + (n & 3) : 32 + 2 * (n >> 2) + (n & 3), kk = 16 + 4 * (g & 1) + j;
        const double wm = b == 0 ? 1 : 2, ws = kk < 20 ? 2 : kk == 20 ? 1 : 0;
        t.v[T_GA + i][l][j] = pk(wm * cs(d1 * b, 40), -wm * sn(d1 * b, 40));
        const u32 gs = pk(ws * cs(d1 * kk, 40), -ws * sn(d1 * kk, 40));
        t.v[T_GS0 + i][l][j] = g < 2 ? gs : 0; t.v[T_GS1 + i][l][j] = g < 2 ? 0 : gs;
      }
    }
    const int d = 32 + 2 * g;
    const u32 mr = pk(cs(d * n, 40), cs((d + 1) * n, 40)), mi = pk(-sn(d * n, 40), -sn((d + 1) * n, 40));
    const u32 sr = pk(cs(d * ks, 40), cs((d + 1) * ks, 40)), si = pk(-sn(d * ks, 40), -sn((d + 1) * ks, 40));
    t.v[T_K16R][l][0] = mr; t.v[T_K16R][l][3] = mr; t.v[T_K16I][l][0] = mi; t.v[T_K16I][l][3] = mi;
    t.v[T_K16S][l][0] = lo ? sr : 0; t.v[T_K16S][l][1] = lo ? 0 : sr; t.v[T_K16S][l][2] = lo ? si : 0; t.v[T_K16S][l][3] = lo ? 0 : si;
  }
  return t;
}
static __device__ const Tab TAB = make_tab();

DI u32 cvtpk(float lo, float hi) { f32x2 v = {lo, hi}; bf16x2 b = __builtin_convertvector(v, bf16x2); return __builtin_bit_cast(u32, b); }
DI float bflo(u32 u) { return __builtin_bit_cast(float, u << 16); }
DI float bfhi(u32 u) { return __builtin_bit_cast(float, u & 0xffff0000u); }
DI float rbf(float x) { return bflo(cvtpk(x, x)); }
DI u32 nosent(u32 v) { return v < 0xfffffffeu ? v : 0xfffffffeu; }
DI u32 max3u(u32 a, u32 b, u32 c) { u32 m = a > b ? a : b; return m > c ? m : c; }
DI u32 tagdiff(u32 m, u32x4 v, u32 tag) { return m | (v[1] ^ tag) | (v[3] ^ tag); }
DI bool any_set(u32 m) { return __builtin_amdgcn_ballot_w64(m != 0) != 0; }
DI bool any_marker(u32 m) { return __builtin_amdgcn_ballot_w64(m == 0xffffffffu) != 0; }

DI u32x4 ld4(rsrc_t r, u32 off, u32 so = 0) { return __builtin_amdgcn_raw_buffer_load_b128(r, off, so, 0); }
DI u32x2 ld2(rsrc_t r, u32 off, u32 so = 0) { return __builtin_amdgcn_raw_buffer_load_b64(r, off, so, 0); }
DI u32x4 ld4n(rsrc_t r, u32 off, u32 so = 0) { return __builtin_amdgcn_raw_buffer_load_b128(r, off, so, NT); }
DI u32x3 ld3n(rsrc_t r, u32 off, u32 so = 0) { return __builtin_amdgcn_raw_buffer_load_b96(r, off, so, NTF); }
DI u32x4 ld4s(rsrc_t r, u32 off, u32 so = 0) { return __builtin_amdgcn_raw_buffer_load_b128(r, off, so, NTS); }
DI u32x2 ld2n(rsrc_t r, u32 off, u32 so = 0) { return __builtin_amdgcn_raw_buffer_load_b64(r, off, so, NT); }
DI u32x4 ld4c(rsrc_t r, u32 off, u32 so = 0) { return __builtin_amdgcn_raw_buffer_load_b128(r, off, so, SC1); }
DI u32x3 ld3c(rsrc_t r, u32 off, u32 so = 0) { return __builtin_amdgcn_raw_buffer_load_b96(r, off, so, SC1); }
DI u32x2 ld2c(rsrc_t r, u32 off, u32 so = 0) { return __builtin_amdgcn_raw_buffer_load_b64(r, off, so, SC1); }
DI u32 ld1c(rsrc_t r, u32 off, u32 so = 0) { return __builtin_amdgcn_raw_buffer_load_b32(r, off, so, SC1); }
DI void st4(rsrc_t r, u32 off, u32 so, u32x4 v) { __builtin_amdgcn_raw_buffer_store_b128(v, r, off, so, SC1); }
DI void st2(rsrc_t r, u32 off, u32 so, u32x2 v) { __builtin_amdgcn_raw_buffer_store_b64(v, r, off, so, SC1); }
DI void st1(rsrc_t r, u32 off, u32 so, u32 v) { __builtin_amdgcn_raw_buffer_store_b32(v, r, off, so, SC1); }
template <class T> DI T lds_ld(u32 a) { return *(const LAS T*)(size_t)a; }
template <class T> DI void lds_st(u32 a, T v) { *(LAS T*)(size_t)a = v; }
#define WAIT_VM(n) asm volatile("s_waitcnt vmcnt(%c0)" ::"i"(n) : "memory")
#define FENCE() asm volatile("" ::: "memory")
#define PIN(x) asm volatile("" : "+v"(x))
#define PINS(x) asm volatile("" : "+s"(x))
DI void bar() { asm volatile("s_waitcnt lgkmcnt(0)\n\ts_barrier" ::: "memory"); }

DI void swap32u(u32& a, u32& b) { auto r = __builtin_amdgcn_permlane32_swap(a, b, false, false); a = r[0]; b = r[1]; }
DI void swap16u(u32& a, u32& b) { auto r = __builtin_amdgcn_permlane16_swap(a, b, false, false); a = r[0]; b = r[1]; }
DI void swap32(float& a, float& b) { u32 x = __builtin_bit_cast(u32, a), y = __builtin_bit_cast(u32, b); swap32u(x, y); a = __builtin_bit_cast(float, x); b = __builtin_bit_cast(float, y); }
DI void swap16(float& a, float& b) { u32 x = __builtin_bit_cast(u32, a), y = __builtin_bit_cast(u32, b); swap16u(x, y); a = __builtin_bit_cast(float, x); b = __builtin_bit_cast(float, y); }
#define ROR(n) "s_nop 1\n\tv_add_f32_dpp %0, %0, %0 row_ror:" #n " row_mask:0xf bank_mask:0xf\n\t"
DI float row_sum(float v) { asm(ROR(8) ROR(4) ROR(2) ROR(1) "s_nop 0" : "+v"(v)); return v; }
#define ROR2(n) "v_add_f32_dpp %0, %0, %0 row_ror:" #n " row_mask:0xf bank_mask:0xf\n\tv_add_f32_dpp %1, %1, %1 row_ror:" #n " row_mask:0xf bank_mask:0xf\n\ts_nop 0\n\t"
DI void row_sum2(float& a, float& b) { asm("s_nop 1\n\t" ROR2(8) ROR2(4) ROR2(2) ROR2(1) : "+v"(a), "+v"(b)); }
DI void wave_sum2(float a, float b, float& sa, float& sb) {
  swap32(a, b); float v = a + b, u = v; swap16(v, u); v = row_sum(v + u);
  sa = __builtin_bit_cast(float, __builtin_amdgcn_readlane(__builtin_bit_cast(int, v), 0));
  sb = __builtin_bit_cast(float, __builtin_amdgcn_readlane(__builtin_bit_cast(int, v), 32));
}

#define M32 "v_mfma_f32_16x16x32_bf16 "
#define M16 "v_mfma_f32_16x16x16_bf16 "
template <bool DA> DI void mm3(f32x4 (&re)[3], f32x4 (&im)[3], const u32x4 (&d)[3], u32x4 fr, u32x4 fi) {
  if (DA)
    asm volatile("s_nop 1\n\t" M32 "%0, %6, %9, 0\n\t" M32 "%3, %6, %10, 0\n\t" M32 "%1, %7, %9, 0\n\t" M32 "%4, %7, %10, 0\n\t" M32 "%2, %8, %9, 0\n\t" M32 "%5, %8, %10, 0\n\ts_nop 7"
                 : "=&v"(re[0]), "=&v"(re[1]), "=&v"(re[2]), "=&v"(im[0]), "=&v"(im[1]), "=&v"(im[2]) : "v"(d[0]), "v"(d[1]), "v"(d[2]), "v"(fr), "v"(fi));
  else
    asm volatile("s_nop 1\n\t" M32 "%0, %9, %6, 0\n\t" M32 "%3, %10, %6, 0\n\t" M32 "%1, %9, %7, 0\n\t" M32 "%4, %10, %7, 0\n\t" M32 "%2, %9, %8, 0\n\t" M32 "%5, %10, %8, 0\n\ts_nop 7"
                 : "=&v"(re[0]), "=&v"(re[1]), "=&v"(re[2]), "=&v"(im[0]), "=&v"(im[1]), "=&v"(im[2]) : "v"(d[0]), "v"(d[1]), "v"(d[2]), "v"(fr), "v"(fi));
}
DI void mm6k16(f32x4 (&o)[6], u32x2 m, const u32x2 (&d)[6]) {
  asm volatile("s_nop 1\n\t" M16 "%0, %6, %7, 0\n\t" M16 "%1, %6, %8, 0\n\t" M16 "%2, %6, %9, 0\n\t" M16 "%3, %6, %10, 0\n\t" M16 "%4, %6, %11, 0\n\t" M16 "%5, %6, %12, 0\n\ts_nop 7"
               : "=&v"(o[0]), "=&v"(o[1]), "=&v"(o[2]), "=&v"(o[3]), "=&v"(o[4]), "=&v"(o[5])
               : "v"(m), "v"(d[0]), "v"(d[1]), "v"(d[2]), "v"(d[3]), "v"(d[4]), "v"(d[5]));
}
DI void mm4k16(f32x4 (&o)[4], u32x2 m, const u32x2 (&d)[4]) {
  asm volatile("s_nop 1\n\t" M16 "%0, %4, %5, 0\n\t" M16 "%1, %4, %6, 0\n\t" M16 "%2, %4, %7, 0\n\t" M16 "%3, %4, %8, 0\n\ts_nop 7"
               : "=&v"(o[0]), "=&v"(o[1]), "=&v"(o[2]), "=&v"(o[3]) : "v"(m), "v"(d[0]), "v"(d[1]), "v"(d[2]), "v"(d[3]));
}
template <int SG> DI u32x4 cmul_pack(f32x4 re, f32x4 im, f32x4 c, f32x4 s) {
  f32x4 a, b;
  if (SG > 0) { a = re * c - im * s; b = im * c + re * s; } else { a = re * c + im * s; b = im * c - re * s; }
  return u32x4{cvtpk(a[0], b[0]), cvtpk(a[1], b[1]), cvtpk(a[2], b[2]), cvtpk(a[3], b[3])};
}

#define ACLOB "a0","a1","a2","a3","a4","a5","a6","a7","a8","a9","a10","a11","a12","a13","a14","a15","a16","a17","a18","a19","a20","a21","a22","a23","a24","a25","a26","a27","a28","a29","a30","a31","a32","a33","a34","a35","a36","a37","a38","a39","a40","a41","a42","a43","a44","a45","a46","a47","a48","a49","a50","a51","a52","a53","a54","a55","a56","a57","a58","a59","a60","a61","a62","a63","a64","a65","a66","a67","a68","a69","a70","a71","a72","a73","a74","a75","a76","a77","a78","a79","a80","a81","a82","a83","a84","a85","a86","a87","a88","a89","a90","a91","a92","a93","a94","a95","a96","a97","a98","a99","a100","a101","a102","a103","a104","a105","a106","a107","a108","a109","a110","a111","a112","a113","a114","a115","a116","a117","a118","a119","a120","a121","a122","a123","a124","a125","a126","a127","a128","a129","a130","a131","a132","a133","a134","a135","a136","a137","a138","a139","a140","a141","a142","a143","a144","a145","a146","a147","a148","a149","a150","a151","a152","a153","a154","a155","a156","a157","a158","a159","a160","a161","a162","a163","a164","a165","a166","a167","a168","a169","a170","a171","a172","a173","a174","a175","a176","a177","a178","a179","a180","a181","a182","a183","a184","a185","a186","a187","a188","a189","a190","a191","a192","a193","a194","a195","a196","a197","a198","a199","a200","a201","a202","a203","a204","a205","a206","a207","a208","a209","a210","a211","a212","a213","a214","a215","a216","a217","a218","a219","a220","a221","a222","a223","a224","a225","a226","a227","a228","a229","a230","a231","a232","a233","a234","a235","a236","a237","a238","a239"
#define PREW 10
template <int Q, int IMM> DI void load_a(u32 voff, rsrc_t rs, u32 soff) {
  asm volatile("buffer_load_dwordx4 a[%c3:%c4], %0, %1, %2 offen offset:%c5 sc1" ::"v"(voff), "s"(rs), "s"(soff), "i"(4 * Q), "i"(4 * Q + 3), "i"(IMM) : "memory", ACLOB);
}
template <int Q0, int Q1> DI void load_w1r(u32 voff, rsrc_t rs, u32 base) {
  if constexpr (Q0 < Q1 && Q0 < 6 * PREW) { load_a<Q0, (Q0 & 3) * 1024>(voff, rs, base + (Q0 >> 2) * 4096); load_w1r<Q0 + 1, Q1>(voff, rs, base); }
}
constexpr int cmin(int a, int b) { return a < b ? a : b; }
#define W1PLAN {2,1,1,1, 2,1,1,1, 2,1,1,1, 2,1,1,1, 2,1,2,2,  2,2,2,1,1,1,2,2,2,2,1,1,1,2,1,1,1,2,2,0,0,0, 4}
constexpr int w1_cnt[43] = W1PLAN;
constexpr int w1_first(int p) { int s = 0; for (int i = 0; i < p; ++i) s += w1_cnt[i]; return cmin(s, 6 * PREW); }
static_assert(w1_first(43) == 6 * PREW, "plan must cover the preloaded fragments");
#define W1(p) load_w1r<w1_first(p), w1_first(p + 1)>(wv, rS, w1base)
DI u32 rot10(u32 c, u32 rot) { const u32 x = c + rot; return x >= 10 ? x - 10 : x; }
template <int C, int F0 = 0, int F1 = 6> DI void load_w1(u32 voff, rsrc_t rs, u32 base, u32 rot) {
  if (C >= PREW) return;
  const u32 s0 = base + rot10(C, rot) * 6144, s1 = s0 + 4096;
  if (F0 <= 0 && 0 < F1) load_a<6 * C + 0, 0>(voff, rs, s0);
  if (F0 <= 1 && 1 < F1) load_a<6 * C + 1, 1024>(voff, rs, s0);
  if (F0 <= 2 && 2 < F1) load_a<6 * C + 2, 2048>(voff, rs, s0);
  if (F0 <= 3 && 3 < F1) load_a<6 * C + 3, 3072>(voff, rs, s0);
  if (F0 <= 4 && 4 < F1) load_a<6 * C + 4, 0>(voff, rs, s1);
  if (F0 <= 5 && 5 < F1) load_a<6 * C + 5, 1024>(voff, rs, s1);
}
DI void dma2(u32 voff, rsrc_t rs, u32 soff, u32 ldsa) {
  asm volatile("s_mov_b32 m0, %3\n\ts_nop 0\n\tbuffer_load_dwordx4 %0, %1, %2 offen sc1 lds\n\tbuffer_load_dwordx4 %0, %1, %2 offen offset:1024 sc1 lds"
               ::"v"(voff), "s"(rs), "s"(soff), "s"(ldsa) : "memory");
}
DI void dma2p(u32 voff, rsrc_t rs, u32 soff, u32 ldsa) {
  asm volatile("s_mov_b32 m0, %3\n\ts_nop 0\n\tbuffer_load_dwordx4 %0, %1, %2 offen lds\n\tbuffer_load_dwordx4 %0, %1, %2 offen offset:1024 lds"
               ::"v"(voff), "s"(rs), "s"(soff), "s"(ldsa) : "memory");
}
DI void dma1(u32 voff, rsrc_t rs, u32 soff, u32 ldsa) {
  asm volatile("s_mov_b32 m0, %3\n\ts_nop 0\n\tbuffer_load_dwordx4 %0, %1, %2 offen lds" ::"v"(voff), "s"(rs), "s"(soff), "s"(ldsa) : "memory");
}

DI u32x2 tr_read(u32 a) { u32x2 r; asm volatile("ds_read_b64_tr_b16 %0, %1" : "=v"(r) : "v"(a) : "memory"); return r; }

template <int R> DI void strip_fma(f32x2 (&a)[4], const u32x4 (&wv)[R], const float (&sc)[R]) {
#pragma unroll
  for (int j = 0; j < 4; ++j) a[j] = f32x2{0, 0};
#pragma unroll
  for (int r = 0; r < R; ++r)
#pragma unroll
    for (int j = 0; j < 4; ++j) a[j] += sc[r] * f32x2{bflo(wv[r][j]), bfhi(wv[r][j])};
}
DI void strip_fold(const f32x2 (&a)[4], float& u, float& v) {
  float s[4];
#pragma unroll
  for (int j = 0; j < 4; ++j) { float x = a[j >> 1][j & 1], y = a[2 + (j >> 1)][j & 1]; swap32(x, y); s[j] = x + y; }
  swap16(s[0], s[2]); swap16(s[1], s[3]);
  u = s[0] + s[2]; v = s[1] + s[3];
}
DI void row_sum2q(float& a, float& b) { asm("s_nop 1\n\t" ROR2(8) ROR2(4) : "+v"(a), "+v"(b)); }
DI u32 mod_pack(float a, float b, bool scale) { const u32 v = cvtpk(a, b); return scale ? cvtpk(1.f + bflo(v), 1.f + bfhi(v)) : v; }
DI float silu_bf(u32 cb) { const float c = bflo(cb); return rbf(c * __builtin_amdgcn_rcpf(1.f + __expf(-c))); }
DI void ln_stats(f32x2 (&v)[2][5], float (&rs)[2], float (&ms)[2]) {
  float t[4], pv[2];
#pragma unroll
  for (int e = 0; e < 2; ++e) {
    pv[e] = __builtin_bit_cast(float, __builtin_amdgcn_readfirstlane(__builtin_bit_cast(int, v[e][0][0])));
#pragma unroll
    for (int i = 0; i < 5; ++i) v[e][i] -= pv[e];
    const f32x2 s = (v[e][0] + v[e][1]) + (v[e][2] + v[e][3]) + v[e][4];
    const f32x2 q = (v[e][0] * v[e][0] + v[e][1] * v[e][1]) + (v[e][2] * v[e][2] + v[e][3] * v[e][3]) + v[e][4] * v[e][4];
    t[e] = s[0] + s[1]; t[2 + e] = q[0] + q[1];
  }
  swap32(t[0], t[2]); swap32(t[1], t[3]);
  float a = t[0] + t[2], b = t[1] + t[3];
  swap16(a, b);
  const int r = __builtin_bit_cast(int, row_sum(a + b));
  const f32x2 m = f32x2{__builtin_bit_cast(float, __builtin_amdgcn_readlane(r, 0)), __builtin_bit_cast(float, __builtin_amdgcn_readlane(r, 16))} * (1.f / 640);
  f32x2 q = f32x2{__builtin_bit_cast(float, __builtin_amdgcn_readlane(r, 32)), __builtin_bit_cast(float, __builtin_amdgcn_readlane(r, 48))} * (1.f / 640) - m * m;
  u32 p = cvtpk(fmaxf(q[0], 0.f), fmaxf(q[1], 0.f));
  p = cvtpk(bflo(p) + 1e-5f, bfhi(p) + 1e-5f);
  p = cvtpk(__builtin_amdgcn_sqrtf(bflo(p)), __builtin_amdgcn_sqrtf(bfhi(p)));
  rs[0] = __builtin_amdgcn_rcpf(bflo(p)); rs[1] = __builtin_amdgcn_rcpf(bfhi(p));
  p = cvtpk(m[0] + pv[0], m[1] + pv[1]);
  ms[0] = (pv[0] - bflo(p)) * rs[0]; ms[1] = (pv[1] - bfhi(p)) * rs[1];
}
DI void mod_scatter(u32 gp, u32x4 g) {
  const u32 vec = (gp * 410) >> 16, d = 4 * (gp - vec * 160), d1 = d >> 4, d2 = d & 15, ex = d1 >= 32;
  const u32 a = L_MV + vec * 2560 + (ex ? 2048 + (((d1 - 32) >> 1) * 16 + d2) * 8 + (d1 & 1) * 4 : ((d1 >> 3) * 16 + d2) * 32 + (d1 & 7) * 4), st = ex ? 8 : 32;
  lds_st<float>(a, bflo(g[0])); lds_st<float>(a + st, bfhi(g[0])); lds_st<float>(a + 2 * st, bflo(g[2])); lds_st<float>(a + 3 * st, bfhi(g[2]));
}
DI void mod_read(f32x2 (&m)[5], u32 vec, u32 lane) {
  const f32x4 a = lds_ld<f32x4>(L_MV + vec * 2560 + lane * 32), b = lds_ld<f32x4>(L_MV + vec * 2560 + lane * 32 + 16);
  m[0] = f32x2{a[0], a[1]}; m[1] = f32x2{a[2], a[3]}; m[2] = f32x2{b[0], b[1]}; m[3] = f32x2{b[2], b[3]};
  m[4] = lds_ld<f32x2>(L_MV + vec * 2560 + 2048 + lane * 8);
}
#define PIN5(x) asm volatile("" : "+v"(x[0]), "+v"(x[1]), "+v"(x[2]), "+v"(x[3]), "+v"(x[4]))

#define STAMP(i) do {} while (0)
#define DLY0 8
#define DLYS 24
#define DLYR 10
#define DLY1 2
#define DLY2 4
#define DLY3 6
#define DLYF 0
#define MKH 1
#define XNT ""
#define MK2 1
#define WG1P 1
#define DLYX 5
#define NREP 8
#define DLYP 0
#define WG2P 2
#define H2P 2
#define MKK3 0
#define MAP 0
#define POSF 0
#define XW1 1
#define XW2 1
#define XFL 1
#define XRS 1
#define XMK 1
#define XWG 1
#define STR2(x) #x
#define STR(x) STR2(x)
#define DLYM 0
#define TABQ(T, q) lds_ld<T>(L_TAB + (q) * 1024 + wv)

__global__ void __launch_bounds__(256, 1) fused_kernel(const void* px, const void* pwr, const void* pwi, const void* pwg, const void* pwu, const void* pwd,
                                                       const void* pc, const void* pwm, void* pout, void* pws) {
  STAMP(0);
  u32 junk = 0;
  {
    unsigned long long pcv; u32 len;
    asm volatile("s_getpc_b64 %0\n.Lk_start:\n\ts_mov_b32 %1, .Lk_end-.Lk_start" : "=s"(pcv), "=s"(len));
    const u32 o = ((blockIdx.x >> 3) + (threadIdx.x & 15) * 32) * 128;
    asm volatile("global_load_dword %0, %1, %2" : "=v"(junk) : "v"(o < len ? o : 0), "s"(pcv) : "memory");
  }
  const u32 tid = threadIdx.x & 255, lane = tid & 63, l15 = lane & 15, g = lane >> 4, wv = lane * 16;
  const u32 w = __builtin_amdgcn_readfirstlane(tid >> 6);
  const u32 b = blockIdx.x, xcd = b & 7, rr = b >> 3, pi = 32 * xcd + rr;
  const u32 mt = 4 * (xcd >> 1) + (rr >> 3), nt = 8 * (xcd & 1) + (rr & 7), m2 = mt, half = xcd & 1, ct = rr & 7;
#define BLK_X(m, n) (2 * ((m) >> 2) + ((n) >> 3))
#define BLK_R(m, n) (8 * ((m) & 3) + ((n) & 7))
  const rsrc_t rC = __builtin_amdgcn_make_buffer_rsrc((void*)pc, 0, 640 * 2, 0x00020000);
  const rsrc_t rWm = __builtin_amdgcn_make_buffer_rsrc((void*)pwm, 0, 640 * 3840 * 2, 0x00020000);
  const rsrc_t rX = __builtin_amdgcn_make_buffer_rsrc((void*)px, 0, 2048 * 640 * 2, 0x00020000);
  const rsrc_t rS = __builtin_amdgcn_make_buffer_rsrc(pws, 0, WS_USED, 0x00020000);
  u32 fail = 0;

  const u32 w2 = w & 1;
  const bool crit = rr < 20, rest = rr >= 20 && rr < 30;
  const u32x4 ones = {~0u, ~0u, ~0u, ~0u};
  const rsrc_t rT = __builtin_amdgcn_make_buffer_rsrc((void*)&TAB, 0, T_N * 1024, 0x00020000);
  const u32 sA = 20 * xcd + rr, sB = 10 * xcd + rr - 20, fb = OFF_FLG + b * 64;
  u32x4 wa[10], U[20], c4; u32 cv[5], c1w;
  const u32 rot0 = sA & 7;
  u32 tagv;
  asm volatile("buffer_load_dword %0, off, %1, 0 sc1" : "=v"(tagv) : "s"(rS) : "memory");
  if (__builtin_expect(w == 0, 1)) {
    asm volatile("buffer_load_dwordx4 %0, %2, %3, 0 offen\n\tbuffer_load_dword %1, %2, %3, 0 offen offset:16" : "=&v"(c4), "=&v"(c1w) : "v"(lane * 20), "s"(rC) : "memory");
    if (__builtin_expect(crit, 1)) {
      u32 so = rot0 * 491520 + sA * 16;
#pragma unroll
      for (int j = 0; j < 10; ++j) {
        asm volatile("buffer_load_dwordx4 %0, %1, %2, %3 offen" XNT : "=v"(wa[j]) : "v"(lane * 7680), "s"(rWm), "s"(so) : "memory");
        so += 491520; const u32 t = so - 4915200; so = t < so ? t : so;
      }
    }
  } else {
    __builtin_amdgcn_s_sleep(DLYX);
    const u32 x1o = lane < 16 ? wv + 1024 : wv + 256 * 1280 - 256, x2o = lane < 32 ? wv + 256 * 1280 + 768 : OOB;
    dma1(wv, rX, (pi + 512 * w) * 1280, L_XS + w * XSP); dma1(x1o, rX, (pi + 512 * w) * 1280, L_XS + w * XSP + 1024); dma1(x2o, rX, (pi + 512 * w) * 1280, L_XS + w * XSP + 2048);
    if (w == 1) {
      dma1(wv, rX, pi * 1280, L_XS); dma1(x1o, rX, pi * 1280, L_XS + 1024); dma1(x2o, rX, pi * 1280, L_XS + 2048);
      asm volatile("v_mov_b32 v252, -1\n\tv_mov_b32 v253, -1\n\tv_mov_b32 v254, -1\n\tv_mov_b32 v255, -1\n\ts_sleep " STR(DLYM) ::: "v252", "v253", "v254", "v255");
#pragma unroll
      for (int j = 0; j < 11; ++j) asm volatile("buffer_store_dwordx4 v[252:255], %0, %1, %2 offen sc1" :: "v"(wv), "s"(rS), "s"(OFF_E1 + pi * E1P + j * 1024) : "memory", "v252", "v253", "v254", "v255");
#pragma unroll
      for (int j = 0; j < 11; ++j) dma2p(wv, rT, j * 2048, L_TAB + j * 2048);
    }
  }
  u32x4 pg[5], pu[5], pd[6]; u32x3 fr[4], fi[4];
  u32 o1 = (lane & 7) < 6 ? (lane >> 3) * 16 * 3072 + (lane & 7) * 16 : OOB, o2 = l15 < 10 ? g * 1280 + l15 * 16 : OOB;
  u32 fo = (((lane >> 1) + 32 * w2) * 8 * 321 + (lane & 1) * 5) * 2, ro = (lane >> 2) * 7680 + (lane & 3) * 16;
  asm volatile("" : "+v"(o1), "+v"(o2), "+v"(fo), "+v"(ro));
  if (w >= 2) {
    const rsrc_t rWg = __builtin_amdgcn_make_buffer_rsrc((void*)pwg, 0, 640 * 1536 * 2, 0x00020000);
    const rsrc_t rWu = __builtin_amdgcn_make_buffer_rsrc((void*)pwu, 0, 640 * 1536 * 2, 0x00020000);
    const rsrc_t rWd = __builtin_amdgcn_make_buffer_rsrc((void*)pwd, 0, 640 * 1536 * 2, 0x00020000);
    __builtin_amdgcn_s_sleep(DLYS);
#pragma unroll
    for (int j = 0; j < 5; ++j) {
      const u32 q = 5 * mt + j, d0 = 128 * (q >> 4) + (q & 15);
      const u32 so = d0 * 3072 + nt * 192 + w2 * 96;
      pg[j] = ld4n(rWg, o1, so); pu[j] = ld4n(rWu, o1, so);
    }
#pragma unroll
    for (int j = 0; j < 6; ++j) pd[j] = ld4n(rWd, o2, (48 * (2 * m2 + half) + 24 * w2 + 4 * j) * 1280 + ct * 160);
    const rsrc_t rWr = __builtin_amdgcn_make_buffer_rsrc((void*)pwr, 0, 2048 * 321 * 2, 0x00020000);
    const rsrc_t rWi = __builtin_amdgcn_make_buffer_rsrc((void*)pwi, 0, 2048 * 321 * 2, 0x00020000);
    __builtin_amdgcn_s_sleep(DLYF);
#pragma unroll
    for (int j = 0; j < 4; ++j) {
      const u32 so = (xcd * 321 + 10 * rr) * 2 + ((j + rr) & 3) * 64 * 8 * 642;
      fr[j] = ld3n(rWr, fo, so); fi[j] = ld3n(rWi, fo, so);
    }
  } else if (rest && w == 0) {
#pragma unroll
    for (int j = 0; j < 20; ++j) U[j] = ld4s(rWm, ro, 2560 + sB * 64 + (320 * (sB & 1) + j * 16) * 7680);
  }
  FENCE();
  STAMP(1);
  u32x2 m8f, m8i;
#pragma unroll
  for (int e = 0; e < 2; ++e) {
    const float a = (float)(((l15 >> 1) * (256 * (2 * g + e) + pi)) & 2047) * (1.f / 2048), c = __builtin_amdgcn_cosf(a), s = __builtin_amdgcn_sinf(a);
    m8f[e] = (l15 & 1) ? cvtpk(-s, c) : cvtpk(c, s);
    const float a2 = (float)(((2 * g + e) * (256 * (l15 >> 1) + pi)) & 2047) * (1.f / 2048), c2 = __builtin_amdgcn_cosf(a2), s2 = __builtin_amdgcn_sinf(a2);
    m8i[e] = (l15 & 1) ? cvtpk(s2, c2) : cvtpk(c2, -s2);
  }
  PIN(m8f); PIN(m8i);
  u32 aMain, aShrR, aShrW;
  {
    const u32 cmain = g < 2 ? l15 + 160 * g : 40 - l15 + 40 * (12 - 4 * g);
    const u32 k1s = 16 + (l15 & 7), k1c = k1s > 20 ? 20 : k1s, cshr = g < 2 ? k1c + 160 * g : 40 - k1c + 40 * (12 - 4 * g);
    aMain = L_S3 + w * S3P + cmain * 8; aShrR = L_S3 + w * S3P + cshr * 8 + (l15 >> 3) * 4;
    aShrW = k1s > 20 ? L_S3 + 4 * S3P + lane * 4 : aShrR;
  }
  PIN(aMain); PIN(aShrW);
  if (w == 0) {
    asm volatile("s_waitcnt vmcnt(10)\n\ts_cmp_lg_u32 %2, 0\n\ts_cbranch_scc1 .Lcw_%=\n\ts_waitcnt vmcnt(0)\n.Lcw_%=:" : "+v"(c4), "+v"(c1w) : "s"(__builtin_amdgcn_readfirstlane(rr < 30)) : "memory", "scc");
    const u32 cw[5] = {c4[0], c4[1], c4[2], c4[3], c1w};
#pragma unroll
    for (int j = 0; j < 5; ++j) lds_st<f32x2>(L_SC + lane * 40 + j * 8, f32x2{silu_bf(cw[j] & 0xffffu), silu_bf(cw[j] >> 16)});
  }
  if (w == 0 && crit) {
    float sc[10]; f32x2 a[4] = {{0, 0}, {0, 0}, {0, 0}, {0, 0}}; float u, v;
#pragma unroll
    for (int j = 0; j < 10; ++j) { const u32 jj = j + rot0 >= 10 ? j + rot0 - 10 : j + rot0; sc[j] = lds_ld<float>(L_SC + lane * 4 + jj * 256); }
    PIN5(sc); PIN5((sc + 5));
#define WSTEP(j) asm volatile("s_waitcnt vmcnt(%c5)" : "+v"(wa[j]), "+v"(a[0]), "+v"(a[1]), "+v"(a[2]), "+v"(a[3]) : "i"(9 - j) : "memory"); _Pragma("unroll") for (int i = 0; i < 4; ++i) a[i] += sc[j] * f32x2{bflo(wa[j][i]), bfhi(wa[j][i])};
    WSTEP(0) WSTEP(1) WSTEP(2) WSTEP(3) WSTEP(4) WSTEP(5) WSTEP(6) WSTEP(7) WSTEP(8) WSTEP(9)
    strip_fold(a, u, v); row_sum2(u, v);
    asm volatile("" : "+v"(tagv));
    st2(rS, l15 < NREP ? g * 8 + l15 * 5120 : OOB, OFF_MODC + sA * 32, u32x2{mod_pack(u, v, sA >= 80), tagv + 1});
  }
  asm volatile("s_cmp_eq_u32 %1, 0\n\ts_cbranch_scc1 .Lxw_%=\n\ts_waitcnt vmcnt(16)\n\ts_cmp_lg_u32 %2, 0\n\ts_cbranch_scc1 .Lxw_%=\n\ts_waitcnt vmcnt(0)\n.Lxw_%=:"
               : "+v"(tagv) : "s"(w), "s"(__builtin_amdgcn_readfirstlane(w >= 2)) : "memory", "scc");
  const u32 tag = __builtin_amdgcn_readfirstlane(tagv) + 1;
  asm volatile("" :: "v"(junk), "s"(tag));
  if (w == 1) {
    lds_st<u32>(L_RED + 256, tag);
    if (lane == 0) st1(rS, ((pi & 15) * 16 + (pi >> 4)) * 4, OFF_FA, tag);
  }
  STAMP(2);
  if (w == 0) { while (*(volatile LAS u32*)(size_t)(L_RED + 256) != tag) {} }
  f32x2 xv[2][5], nv[2][5];
  const u32 xsa = L_XS + w * XSP + g * 256 + l15 * 2, xsb = L_XS + w * XSP + 1024 + g * 64 + l15 * 2;
#pragma unroll
  for (int e = 0; e < 2; ++e)
#pragma unroll
    for (int i = 0; i < 5; ++i) {
      const u32 a = (i < 4 ? xsa + i * 64 : xsb) + e * 1280;
      xv[e][i] = f32x2{bflo(lds_ld<unsigned short>(a)), bflo(lds_ld<unsigned short>(a + 32))}; nv[e][i] = xv[e][i];
    }
  {
    float rs[2], ms[2]; ln_stats(nv, rs, ms);
#pragma unroll
    for (int e = 0; e < 2; ++e) {
#pragma unroll
      for (int i = 0; i < 5; ++i) nv[e][i] = nv[e][i] * rs[e] + ms[e];
      PIN5(nv[e]); PIN5(xv[e]);
    }
  }
  STAMP(3);
  STAMP(17);
  while (*(volatile LAS u32*)(size_t)(L_RED + 256) != tag) {}
  STAMP(4);
  u32 e1o = g * 2 * E1R + w * 320, e1x = (w == 3 && l15 < 2) ? e1o + 320 + l15 * 4 : OOB; const u32 e1s = OFF_E1 + pi * E1P;
  PIN(e1o); PIN(e1x);
  u32x4 c1[15];
  const u32 q4 = l15 >> 2, p4 = l15 & 3;
#pragma unroll
  for (int q = 0; q < 15; ++q) c1[q] = TABQ(u32x4, q);
  PIN5(c1); PIN5((c1 + 5)); PIN5((c1 + 10));
  if (w >= 2) {
    const u32 p1 = L_P1 + w2 * 40 * P1P;
    if ((lane & 7) < 6) {
#pragma unroll
      for (int j = 0; j < 5; ++j) {
        const u32 a = p1 + ((lane >> 3) + 8 * j) * P1P + (lane & 7) * 32;
        lds_st<u32x4>(a, u32x4{pg[j][0], pu[j][0], pg[j][1], pu[j][1]}); lds_st<u32x4>(a + 16, u32x4{pg[j][2], pu[j][2], pg[j][3], pu[j][3]});
      }
    }
    const u32 p2 = L_P2 + w2 * 24 * P2P;
    if (l15 < 10) {
#pragma unroll
      for (int j = 0; j < 6; ++j) lds_st<u32x4>(p2 + (g + 4 * j) * P2P + l15 * 16, pd[j]);
    }
    asm volatile("s_waitcnt lgkmcnt(0)" ::: "memory");
    u32x2 t1[8][2];
#pragma unroll
    for (int j = 0; j < 8; ++j) {
      const u32 ci = j < 5 ? j : j < 7 ? g : 4, tl = j < 5 ? g : j < 7 ? j - 1 : 4 + (g & 1), ti = 6 * w2 + tl, wq = 2 * w2 + (tl >= 3), q = 5 * mt + ci;
      const u32 a = p1 + (8 * ci + q4) * P1P + tl * 32 + p4 * 8;
      t1[j][0] = tr_read(a); t1[j][1] = tr_read(a + 4 * P1P);
      U[8 + (j >> 2)][j & 3] = (j == 7 && g >= 2) ? OOB : OFF_WG1 + ((nt * 4 + wq) * 60 + (q >> 2) * 3 + (ti - 3 * wq)) * 1024 + ((q & 3) * 16 + l15) * 16;
    }
#define T2(j) "+v"(t1[j][0]), "+v"(t1[j][1])
    asm volatile("s_waitcnt lgkmcnt(0)" : T2(0), T2(1), T2(2), T2(3), T2(4), T2(5), T2(6), T2(7) :: "memory");
#pragma unroll
    for (int j = 0; j < 8; ++j) { U[j] = u32x4{t1[j][0][0], t1[j][0][1], t1[j][1][0], t1[j][1][1]}; PIN(U[j]); }
    PIN(U[8]); PIN(U[9]);
#pragma unroll
    for (int k = 0; k < 4; ++k) {
      const u32 kk = (lane >> 1) + 32 * w2 + 64 * ((k + rr) & 3), a = L_WL + (lane & 1) * 5 * KIP + ((kk & 15) * 20 + (kk >> 4)) * 4;
#pragma unroll
      for (int j = 0; j < 3; ++j) {
        lds_st<u32>(a + (2 * j) * KIP, __builtin_amdgcn_perm(fi[k][j], fr[k][j], 0x05040100));
        lds_st<u32>(a + (2 * j + 1) * KIP, __builtin_amdgcn_perm(fi[k][j], fr[k][j], 0x07060302));
      }
    }
    STAMP(5);
  } else if (w == 0) {
    u32 ma[5];
#pragma unroll
    for (int j = 0; j < 5; ++j) {
      const u32 gp = lane + 64 * j, vec = gp >= 160, d = 4 * (gp - vec * 160), d1 = d >> 4, d2 = d & 15;
      ma[j] = L_MV + vec * 2560 + (d1 >= 32 ? 2048 + (((d1 - 32) >> 1) * 16 + d2) * 8 + (d1 & 1) * 4 : ((d1 >> 3) * 16 + d2) * 32 + (d1 & 7) * 4);
    }
    u32 s2 = lane < 32 ? 8 : 32;
    PIN5(ma); PIN(s2);
    STAMP(5);
    u32x4 gr[5]; u32 po[5];
    const u32 mso = OFF_MODC + (xcd & (NREP - 1)) * 5120;
#pragma unroll
    for (int j = 0; j < 5; ++j) { po[j] = wv + j * 1024; gr[j] = u32x4{0, 0, 0, 0}; }
    __builtin_amdgcn_s_sleep(DLYP);
    for (u32 sp = 0;; ++sp) {
      FENCE();
      u32x4 t[5]; u32 m = 0;
#pragma unroll
      for (int j = 0; j < 5; ++j) t[j] = ld4c(rS, po[j], mso);
#pragma unroll
      for (int j = 0; j < 5; ++j) {
        const bool act = po[j] != OOB, ok = act && t[j][1] == tag && t[j][3] == tag;
        gr[j][0] = ok ? t[j][0] : gr[j][0]; gr[j][2] = ok ? t[j][2] : gr[j][2];
        po[j] = ok ? OOB : po[j]; m |= po[j] ^ OOB;
      }
      if (!any_set(m)) break;
      if (sp > SPIN_CAP) { fail = 1; break; }
      __builtin_amdgcn_s_sleep(DLY0);
    }
#pragma unroll
    for (int j = 0; j < 5; ++j) {
      const u32 a = ma[j], st = j == 2 ? s2 : j == 4 ? (lane < 32 ? 32 : 8) : 32;
      lds_st<float>(a, bflo(gr[j][0])); lds_st<float>(a + st, bfhi(gr[j][0])); lds_st<float>(a + 2 * st, bflo(gr[j][2])); lds_st<float>(a + 3 * st, bfhi(gr[j][2]));
    }
  } else {
    if (rest) {
#pragma unroll
      for (int j = 0; j < 20; ++j) U[j] = ld4s(rWm, ro, 2560 + sB * 64 + (320 * (~sB & 1) + j * 16) * 7680);
    }
    STAMP(5);
  }
  STAMP(6);
  bar();
  STAMP(7);
  {
    u32x4 a32[2]; u32x2 a16;
    {
      f32x2 sa[5], ca[5]; mod_read(sa, 0, lane); mod_read(ca, 1, lane);
#pragma unroll
      for (int j = 0; j < 3; ++j) st4(rS, tid * 16, OFF_E2 + pi * E2P + j * 4096, ones);
      if (w < 2) {
#pragma unroll
        for (int j = 0; j < 3; ++j) dma2p(wv, rT, (11 + 2 * j + w) * 2048, L_TAB + (11 + 2 * j + w) * 2048);
      } else if (WG1P == 1) {
#pragma unroll
        for (int j = 0; j < 8; ++j) st4(rS, U[8 + (j >> 2)][j & 3], 0, U[j]);
      }
      FENCE();
#pragma unroll
      for (int e = 0; e < 2; ++e) {
        f32x2 h[5];
#pragma unroll
        for (int i = 0; i < 5; ++i) h[i] = nv[e][i] * ca[i] + sa[i];
        a32[e] = u32x4{cvtpk(h[0][0], h[0][1]), cvtpk(h[1][0], h[1][1]), cvtpk(h[2][0], h[2][1]), cvtpk(h[3][0], h[3][1])};
        a16[e] = cvtpk(h[4][0], h[4][1]);
      }
    }
    f32x4 cr[3], ci[3];
    {
      const u32x2 r0 = {c1[T_K16R][0], c1[T_K16R][1]}, r1 = {c1[T_K16R][2], c1[T_K16R][3]}, i0 = {c1[T_K16I][0], c1[T_K16I][1]}, i1 = {c1[T_K16I][2], c1[T_K16I][3]};
      const u32x2 sr = {c1[T_K16S][0], c1[T_K16S][1]}, si = {c1[T_K16S][2], c1[T_K16S][3]};
      asm volatile("s_nop 1\n\t"
                   M32 "%0, %6, %9, 0\n\t" M32 "%3, %6, %10, 0\n\t" M32 "%1, %7, %9, 0\n\t" M32 "%4, %7, %10, 0\n\t"
                   M32 "%2, %6, %11, 0\n\t" M32 "%5, %6, %13, 0\n\t"
                   M16 "%0, %8, %15, %0\n\t" M16 "%3, %8, %17, %3\n\t" M16 "%1, %8, %16, %1\n\t" M16 "%4, %8, %18, %4\n\t"
                   M32 "%2, %7, %12, %2\n\t" M32 "%5, %7, %14, %5\n\t" M16 "%2, %8, %19, %2\n\t" M16 "%5, %8, %20, %5\n\ts_nop 7"
                   : "=&v"(cr[0]), "=&v"(cr[1]), "=&v"(cr[2]), "=&v"(ci[0]), "=&v"(ci[1]), "=&v"(ci[2])
                   : "v"(a32[0]), "v"(a32[1]), "v"(a16), "v"(c1[T_BM32R]), "v"(c1[T_BM32I]), "v"(c1[T_S0R]), "v"(c1[T_S1R]), "v"(c1[T_S0I]), "v"(c1[T_S1I]),
                     "v"(r0), "v"(r1), "v"(i0), "v"(i1), "v"(sr), "v"(si) : "memory");
    }
#define F4(x) __builtin_bit_cast(f32x4, x)
    u32x4 dd[3] = {cmul_pack<-1>(cr[0], ci[0], F4(c1[T_TWC]), F4(c1[T_TWS])), cmul_pack<-1>(cr[1], ci[1], F4(c1[T_TWC]), F4(c1[T_TWS])),
                   cmul_pack<-1>(cr[2], ci[2], F4(c1[T_TSC]), F4(c1[T_TSS]))};
    mm3<false>(cr, ci, dd, c1[T_K1R], c1[T_K1I]);
#pragma unroll
    for (int r = 0; r < 4; ++r) {
      lds_st<u32x2>(aMain + r * 320, u32x2{cvtpk(cr[0][r], ci[0][r]), cvtpk(cr[1][r], ci[1][r])});
      lds_st<u32>(aShrW + r * 320, cvtpk(cr[2][r], ci[2][r]));
    }
    bar();
    u32x2 d8[6]; f32x4 o8[6];
    {
      const u32 a = L_S3 + g * S3P + w * 640;
      const u32x4 v0 = lds_ld<u32x4>(a + l15 * 32), v1 = lds_ld<u32x4>(a + l15 * 32 + 16);
      d8[0] = u32x2{v0[0], v0[1]}; d8[1] = u32x2{v0[2], v0[3]}; d8[2] = u32x2{v1[0], v1[1]}; d8[3] = u32x2{v1[2], v1[3]};
      d8[4] = lds_ld<u32x2>(a + 512 + l15 * 8); d8[5] = lds_ld<u32x2>(a + 640 + l15 * 8);
    }
    mm6k16(o8, m8f, d8);
    u32 v[2][6];
#pragma unroll
    for (int k = 0; k < 2; ++k)
#pragma unroll
      for (int t = 0; t < 6; ++t) v[k][t] = cvtpk(o8[t][2 * k], o8[t][2 * k + 1]);
#pragma unroll
    for (int k = 0; k < 2; ++k) {
      st4(rS, e1o + l15 * 16 + k * E1R, e1s, u32x4{v[k][0], v[k][1], v[k][2], v[k][3]});
      st1(rS, e1o + l15 * 4 + 256 + k * E1R, e1s, v[k][4]);
      st1(rS, e1x + k * E1R, e1s, nosent(v[k][5]));
    }
  }
  STAMP(8);
  const u32 rot = 0, rot2 = 0;
  const u32 w1base = OFF_WG1 + (nt * 4 + w) * 61440;

  {
    u32x4 w2d[4], w2o;
    if (w < 2) WAIT_VM(12); else WAIT_VM(6);
    if (lane == 0) st1(rS, ((rr >> 3) * 64 + xcd * 8 + (rr & 7)) * 16 + w * 4, OFF_FB, tag);
    if (w < 2) {
    } else {
      if (lane == 0) st1(rS, fb + 8 + w2 * 4, 0, tag);
      const u32 p2 = L_P2 + w2 * 24 * P2P;
      u32x2 t1[4][2];
#pragma unroll
      for (int j = 0; j < 4; ++j) {
        const u32 ci = j < 3 ? j : (g < 3 ? g : 2), ti = j < 3 ? g : 4, Q = 6 * (2 * m2 + half) + 3 * w2 + ci, KS = Q >> 2, kq = m2 >> 2;
        const u32 a = p2 + (8 * ci + q4) * P2P + (j < 3 ? (32 * (g >> 1) + 8 * p4 + 4 * (g & 1)) * 2 : (64 + 4 * p4) * 2);
        t1[j][0] = tr_read(a); t1[j][1] = tr_read(a + 4 * P2P);
        w2o[j] = (j == 3 && g == 3) ? OOB : OFF_WG2 + ((ct * 4 + kq) * 60 + (KS - kq * 12) * 5 + ti) * 1024 + ((Q & 3) * 16 + l15) * 16;
      }
      asm volatile("s_waitcnt lgkmcnt(0)" : T2(0), T2(1), T2(2), T2(3) :: "memory");
#pragma unroll
      for (int j = 0; j < 4; ++j) { w2d[j] = u32x4{t1[j][0][0], t1[j][0][1], t1[j][1][0], t1[j][1][1]}; PIN(w2d[j]); }
      PIN(w2o);
    }
    u32x4 c2[6], wf[3]; f32x4 wfr[3], wfi[3];
#pragma unroll
    for (int q = 0; q < 6; ++q) c2[q] = TABQ(u32x4, T_T2C + q);
    u32 go = (16 * ((lane >> 2) & 3) + g + 4 * w) * E1P + (lane & 3) * 12, lo = L_KI + (lane & 3) * 3 * KIP + ((g + 4 * w) * 20 + ((lane >> 2) & 3)) * 4;
    u32 ua = (3 * w) * KIP + l15 * 80 + g * 16, ao = L_KO + (64 * g + l15) * 48 + w * 12;
    PIN(go); PIN(lo); PIN(ua); PIN(ao);
#pragma unroll
    for (int j = 0; j < 3; ++j) wf[j] = lds_ld<u32x4>(L_WL + ua + j * KIP);
    PIN5(c2); PIN(c2[5]);
#pragma unroll
    for (int j = 0; j < 3; ++j) {
#pragma unroll
      for (int r = 0; r < 4; ++r) { wfr[j][r] = bflo(wf[j][r]); wfi[j][r] = bfhi(wf[j][r]); }
      PIN(wfr[j]); PIN(wfi[j]);
    }
    const u32 gso = OFF_E1 + xcd * E1R + rr * 40;
    for (u32 sp = 0;; ++sp) {
      FENCE();
      if (!any_set(ld1c(rS, lane * 4, OFF_FA + w * 256) ^ tag)) break;
      if (sp > SPIN_CAP || fail) { fail = 1; break; }
    }
    __builtin_amdgcn_s_sleep(DLY1);
    STAMP(9);
    u32x3 v[4];
#pragma unroll
    for (int j = 0; j < 4; ++j) v[j] = ld3c(rS, go, gso + j * 64 * E1P);
    FENCE();
    if (w < 2 && rest) {
      {
        float sc[20]; f32x2 a[4]; float u, v;
#pragma unroll
        for (int j = 0; j < 20; ++j) sc[j] = lds_ld<float>(L_SC + (w2 ^ (sB & 1)) * 1280 + (lane >> 2) * 4 + j * 64);
        strip_fma<20>(a, U, sc); strip_fold(a, u, v); row_sum2q(u, v);
        if (l15 < 4) lds_st<f32x2>(L_RED + (w2 * 16 + l15 * 4 + g) * 8, f32x2{u, v});
      }
    }
    for (u32 sp = 0;; ++sp) {
      u32 m = max3u(v[0][0], v[0][1], v[0][2]);
      m = max3u(m, v[1][0], v[1][1]); m = max3u(m, v[1][2], v[2][0]); m = max3u(m, v[2][1], v[2][2]); m = max3u(m, v[3][0], v[3][1]); m = m > v[3][2] ? m : v[3][2];
      if (!any_marker(m)) break;
      if (sp > SPIN_CAP || fail) { fail = 1; break; }
      FENCE();
#pragma unroll
      for (int j = 0; j < 4; ++j) v[j] = ld3c(rS, go, gso + j * 64 * E1P);
    }
    STAMP(10);
    const u32x2 f1 = ld2c(rS, (BLK_X(l15, nt) + 8 * BLK_R(l15, nt)) * 64 + 8, OFF_FLG);
#pragma unroll
    for (int j = 0; j < 4; ++j) { lds_st<u32>(lo + j * 16, v[j][0]); lds_st<u32>(lo + j * 16 + KIP, v[j][1]); lds_st<u32>(lo + j * 16 + 2 * KIP, v[j][2]); }
    bar();
    STAMP(11);
    if (rest && w == 3 && lane < 16) {
      const f32x2 p = lds_ld<f32x2>(L_RED + lane * 8) + lds_ld<f32x2>(L_RED + 128 + lane * 8);
      st2(rS, lane * 8, OFF_MODG + 5120 + sB * 128, u32x2{mod_pack(p[0], p[1], sB >= 40 && sB < 60), tag});
    }
    u32x4 dd[3]; f32x4 cr[3], ci[3];
#pragma unroll
    for (int j = 0; j < 3; ++j) dd[j] = lds_ld<u32x4>(L_KI + ua + j * KIP);
    if (H2P == 2 && w < 2) {
#pragma unroll
      for (int j = 0; j < 5; ++j) st4(rS, wv, OFF_H2 + mt * 163840 + nt * 1024 + (2 * j + w) * 16384, ones);
    }
    if (WG2P == 2 && w >= 2) {
#pragma unroll
      for (int j = 0; j < 4; ++j) st4(rS, w2o[j], 0, w2d[j]);
    }
    FENCE();
    mm3<true>(cr, ci, dd, c2[2], c2[3]);
    if (__builtin_expect(any_set((f1[0] ^ tag) | (f1[1] ^ tag)), 0)) {
      for (u32 sp = 0;; ++sp) {
        FENCE();
        const u32x2 f = ld2c(rS, (BLK_X(l15, nt) + 8 * BLK_R(l15, nt)) * 64 + 8, OFF_FLG);
        if (!any_set((f[0] ^ tag) | (f[1] ^ tag))) break;
        if (sp > SPIN_CAP || fail) { fail = 1; break; }
      }
    }
#define CM(P, SG, C0, S0, C1, S1, C2, S2) \
    W1(P); dd[0] = cmul_pack<SG>(cr[0], ci[0], C0, S0); PIN(dd[0]); W1(P + 1); dd[1] = cmul_pack<SG>(cr[1], ci[1], C1, S1); PIN(dd[1]); \
    W1(P + 2); dd[2] = cmul_pack<SG>(cr[2], ci[2], C2, S2); PIN(dd[2]); W1(P + 3);
    CM(0, -1, F4(c2[0]), F4(c2[1]), F4(c2[0]), F4(c2[1]), F4(c2[0]), F4(c2[1]))
    mm3<false>(cr, ci, dd, c2[2], c2[3]);
    CM(4, 1, wfr[0], wfi[0], wfr[1], wfi[1], wfr[2], wfi[2])
    mm3<true>(cr, ci, dd, c2[4], c2[5]);
    CM(8, 1, F4(c2[0]), F4(c2[1]), F4(c2[0]), F4(c2[1]), F4(c2[0]), F4(c2[1]))
    mm3<false>(cr, ci, dd, c2[4], c2[5]);
#define KO(r) _Pragma("unroll") for (int j = 0; j < 3; ++j) lds_st<u32>(ao + r * 768 + j * 4, j < 2 ? cvtpk(cr[j][r], ci[j][r]) : nosent(cvtpk(cr[j][r], ci[j][r])));
    W1(12); KO(0) W1(13); KO(1) W1(14); KO(2) W1(15); KO(3)
    WAIT_VM(w1_first(16));
    W1(16);
    bar();
    u32x4 ov[3];
#pragma unroll
    for (int j = 0; j < 3; ++j) ov[j] = lds_ld<u32x4>(L_KO + tid * 16 + j * 4096);
    W1(17);
#pragma unroll
    for (int j = 0; j < 3; ++j) st4(rS, tid * 16, OFF_E2 + pi * E2P + j * 4096, ov[j]);
  }
  STAMP(12);
  if (b == 0 && tid == 0) st1(rS, OFF_EPOCH, 0, tag);

  u32x4 x1r[3];
  {
    W1(18); W1(19);
    u32x4 gr[3], f2 = ld4c(rS, wv, OFF_FB + w * 1024);
    const u32 mo = tid < 128 ? tid * 16 + 8192 : tid * 16;
    gr[0] = ld4c(rS, tid * 16, OFF_MODG + 5120); gr[1] = ld4c(rS, tid * 16, OFF_MODG + 5120 + 4096); gr[2] = ld4c(rS, mo, OFF_MODG + 5120);
    u32x4 c3[15];
#pragma unroll
    for (int q = 0; q < 13; ++q) c3[q] = TABQ(u32x4, T_IAR + q);
    c3[13] = TABQ(u32x4, T_TWC); c3[14] = TABQ(u32x4, T_TWS);
    u32 go[2], lw[2], rm, rm3, rsx;
#pragma unroll
    for (int i = 0; i < 2; ++i) {
      const u32 s0 = 16 * i + l15, sg = s0 < 24 ? s0 : l15, pc = (sg * 43) >> 7, ch = sg - 3 * pc;
      go[i] = (64 * g + 8 * w + pc) * E2P + ch * 16;
      lw[i] = s0 < 24 ? L_S3 + 2 * g * S4P + (24 * w + sg) * 16 : L_S3 + 8 * S4P + l15 * 16 - S4P * 0;
    }
    {
      const u32 k1c = 16 + ((l15 & 7) > 4 ? 4 : (l15 & 7));
      const u32 cm = g < 2 ? l15 : 40 - l15, cs = g < 2 ? k1c : 40 - k1c, kb = g < 2 ? 4 * g : 12 - 4 * g;
      rm = L_S3 + 2 * w * S4P + (cm + 2 * ((cm * 26) >> 8) + 48 * kb) * 4;
      rm3 = rm + 576 - (lane == 32 ? 8 : 0);
      rsx = L_S3 + (2 * w + (l15 >> 3)) * S4P + (cs + 2 * ((cs * 26) >> 8) + 48 * kb) * 4;
    }
    u32 ho4[2], ho1[2];
#pragma unroll
    for (int e = 0; e < 2; ++e) {
      ho4[e] = (lane >> 3) * 16384 + (((lane & 7) ^ (2 * w + e)) * 16) + e * 128;
      ho1[e] = (8 + (l15 >> 3)) * 16384 + (((l15 & 7) ^ (2 * w + e)) * 16) + g * 4 + e * 128;
      PIN(ho4[e]); PIN(ho1[e]);
    }
    PIN(go[0]); PIN(go[1]); PIN(lw[0]); PIN(lw[1]); PIN(rm); PIN(rm3); PIN(rsx);
    PIN5(c3); PIN5((c3 + 5)); PIN5((c3 + 10));
    for (u32 sp = 0; any_set(tagdiff(tagdiff(tagdiff(tagdiff((f2[0] ^ tag) | (f2[2] ^ tag), f2, tag), gr[0], tag), gr[1], tag), gr[2], tag)); ++sp) {
      if (sp > SPIN_CAP || fail) { fail = 1; break; }
      FENCE();
      f2 = ld4c(rS, wv, OFF_FB + w * 1024);
      gr[0] = ld4c(rS, tid * 16, OFF_MODG + 5120); gr[1] = ld4c(rS, tid * 16, OFF_MODG + 5120 + 4096); gr[2] = ld4c(rS, mo, OFF_MODG + 5120);
    }
    const u32 gso = OFF_E2 + 48 * pi;
    __builtin_amdgcn_s_sleep(DLY2);
    STAMP(13);
    u32x4 v[2][2];
#pragma unroll
    for (int i = 0; i < 2; ++i) { v[i][0] = ld4c(rS, go[i], gso); v[i][1] = ld4c(rS, go[i], gso + 32 * E2P); }
    FENCE();
    mod_scatter(320 + tid, gr[0]); mod_scatter(576 + tid, gr[1]); mod_scatter(320 + (mo >> 4), gr[2]);
    bar();
    f32x2 ga[5], sb[5], cb[5];
    mod_read(ga, 2, lane); mod_read(sb, 3, lane); mod_read(cb, 4, lane);
#pragma unroll
    for (int i = 0; i < 5; ++i) ga[i] *= 1.f / (2048.f * 640.f);
    PIN5(ga); PIN5(sb); PIN5(cb);
    for (u32 sp = 0;; ++sp) {
      u32 m = 0;
#pragma unroll
      for (int i = 0; i < 4; ++i) { m = max3u(m, v[i >> 1][i & 1][0], v[i >> 1][i & 1][1]); m = max3u(m, v[i >> 1][i & 1][2], v[i >> 1][i & 1][3]); }
      if (!any_marker(m)) break;
      if (sp > SPIN_CAP || fail) { fail = 1; break; }
      FENCE();
#pragma unroll
      for (int i = 0; i < 2; ++i) { v[i][0] = ld4c(rS, go[i], gso); v[i][1] = ld4c(rS, go[i], gso + 32 * E2P); }
    }
    STAMP(14);
    const u32 so = OFF_H2 + mt * 163840 + nt * 1024 + w * 256;
#pragma unroll
    for (int j = 0; j < MKK3; ++j) st4(rS, wv, OFF_H + (mt * 16 + nt) * 24576 + (24 - 4 * MKK3 + w * MKK3 + j) * 1024, ones);
    FENCE();
    {
      f32x4 o[8];
      u32x2 p[8];
#pragma unroll
      for (int i = 0; i < 2; ++i) {
        u32 a0 = v[i][0][0], a1 = v[i][0][1], a2 = v[i][0][2], a3 = v[i][0][3], b0 = v[i][1][0], b1 = v[i][1][1], b2 = v[i][1][2], b3 = v[i][1][3];
        asm volatile("v_swap_b32 %0, %1\n\tv_swap_b32 %2, %3" : "+v"(a1), "+v"(b0), "+v"(a3), "+v"(b2));
        p[4 * i] = u32x2{a0, a1}; p[4 * i + 1] = u32x2{b0, b1}; p[4 * i + 2] = u32x2{a2, a3}; p[4 * i + 3] = u32x2{b2, b3};
      }
      asm volatile("s_nop 1\n\t" M16 "%0, %8, %9, 0\n\t" M16 "%1, %8, %10, 0\n\t" M16 "%2, %8, %11, 0\n\t" M16 "%3, %8, %12, 0\n\t"
                   M16 "%4, %8, %13, 0\n\t" M16 "%5, %8, %14, 0\n\t" M16 "%6, %8, %15, 0\n\t" M16 "%7, %8, %16, 0\n\ts_nop 3"
                   : "=&v"(o[0]), "=&v"(o[1]), "=&v"(o[2]), "=&v"(o[3]), "=&v"(o[4]), "=&v"(o[5]), "=&v"(o[6]), "=&v"(o[7])
                   : "v"(m8i), "v"(p[0]), "v"(p[1]), "v"(p[2]), "v"(p[3]), "v"(p[4]), "v"(p[5]), "v"(p[6]), "v"(p[7]));
      const u32x4 q00 = {cvtpk(o[0][0], o[0][1]), cvtpk(o[1][0], o[1][1]), cvtpk(o[2][0], o[2][1]), cvtpk(o[3][0], o[3][1])};
      const u32x4 q01 = {cvtpk(o[0][2], o[0][3]), cvtpk(o[1][2], o[1][3]), cvtpk(o[2][2], o[2][3]), cvtpk(o[3][2], o[3][3])};
      W1(20);
      lds_st<u32x4>(lw[0], q00); lds_st<u32x4>(lw[0] + S4P, q01);
      asm volatile("" : "+v"(o[4]), "+v"(o[5]), "+v"(o[6]), "+v"(o[7]) :: "memory");
      const u32x4 q10 = {cvtpk(o[4][0], o[4][1]), cvtpk(o[5][0], o[5][1]), cvtpk(o[6][0], o[6][1]), cvtpk(o[7][0], o[7][1])};
      const u32x4 q11 = {cvtpk(o[4][2], o[4][3]), cvtpk(o[5][2], o[5][3]), cvtpk(o[6][2], o[6][3]), cvtpk(o[7][2], o[7][3])};
      lds_st<u32x4>(lw[1], q10); lds_st<u32x4>(lw[1] + S4P, q11);
    }
    W1(21);
    bar();
    u32x4 dd[3]; f32x4 cr[3], ci[3];
#pragma unroll
    for (int j = 0; j < 4; ++j) { dd[0][j] = lds_ld<u32>(j < 3 ? rm + j * 192 : rm3); dd[1][j] = lds_ld<u32>((j < 3 ? rm + j * 192 : rm3) + S4P); dd[2][j] = lds_ld<u32>(rsx + j * 192); }
    W1(22);
    mm3<true>(cr, ci, dd, c3[0], c3[1]);
    CM(23, 1, F4(c3[13]), F4(c3[14]), F4(c3[13]), F4(c3[14]), F4(c3[2]), F4(c3[3]))
    f32x4 y[2][3];
    asm volatile("s_nop 1\n\t"
                 M32 "%0, %9, %6, 0\n\t" M32 "%3, %9, %7, 0\n\t" M32 "%1, %10, %6, 0\n\t" M32 "%4, %10, %7, 0\n\t" M32 "%2, %11, %6, 0\n\t" M32 "%5, %11, %7, 0\n\t"
                 M32 "%0, %12, %8, %0\n\t" M32 "%3, %15, %8, %3\n\t" M32 "%1, %13, %8, %1\n\t" M32 "%4, %16, %8, %4\n\t" M32 "%2, %14, %8, %2\n\t" M32 "%5, %17, %8, %5"
                 : "=&v"(y[0][0]), "=&v"(y[0][1]), "=&v"(y[0][2]), "=&v"(y[1][0]), "=&v"(y[1][1]), "=&v"(y[1][2])
                 : "v"(dd[0]), "v"(dd[1]), "v"(dd[2]), "v"(c3[4]), "v"(c3[5]), "v"(c3[6]), "v"(c3[7]), "v"(c3[8]), "v"(c3[9]), "v"(c3[10]), "v"(c3[11]), "v"(c3[12]) : "memory");
    W1(27); W1(28); W1(29);
    asm volatile("s_nop 7" : "+v"(y[0][0]), "+v"(y[0][1]), "+v"(y[0][2]), "+v"(y[1][0]), "+v"(y[1][1]), "+v"(y[1][2]) :: "memory");
    f32x2 x1[2][5]; u32 xp[2][5];
#define RES(e, i) { const f32x2 t = ga[i] * f32x2{y[e][i >> 1][2 * (i & 1)], y[e][i >> 1][2 * (i & 1) + 1]} + xv[e][i]; xp[e][i] = cvtpk(t[0], t[1]); x1[e][i] = f32x2{bflo(xp[e][i]), bfhi(xp[e][i])}; }
    RES(0, 0) RES(0, 1) RES(0, 2) PIN(x1[0][2]); W1(30); RES(0, 3) RES(0, 4) RES(1, 0) PIN(x1[1][0]); W1(31); RES(1, 1) RES(1, 2) PIN(x1[1][2]); W1(32); RES(1, 3) RES(1, 4) PIN(x1[1][4]); W1(33);
    float rs[2], ms[2]; ln_stats(x1, rs, ms);
#pragma unroll
    for (int e = 0; e < 2; ++e) {
      f32x2 h[5];
#pragma unroll
      for (int i = 0; i < 5; ++i) h[i] = x1[e][i] * rs[e] + ms[e];
      PIN5(h);
      if (e == 0) { W1(34); } else { W1(36); }
#pragma unroll
      for (int i = 0; i < 5; ++i) h[i] = h[i] * cb[i] + sb[i];
      if (e == 0) { W1(35); }
      st4(rS, ho4[e], so, u32x4{cvtpk(h[0][0], h[0][1]), cvtpk(h[1][0], h[1][1]), cvtpk(h[2][0], h[2][1]), cvtpk(h[3][0], h[3][1])});
      st1(rS, ho1[e], so, cvtpk(h[4][0], h[4][1]));
    }
    STAMP(15);
    const u32 fl0 = OFF_FLG + ((xcd & 6) + ((lane >> 3) & 1) + 8 * (8 * (mt & 3) + (lane & 7))) * 64 + 16;
    W1(37); W1(38); W1(39); W1(40); W1(41); W1(42);
#pragma unroll
    for (int e = 0; e < 2; ++e)
#pragma unroll
      for (int i = 0; i < 5; ++i) {
        const u32 a = (i < 4 ? xsa + i * 64 : xsb) + e * 1280;
        lds_st<unsigned short>(a, (unsigned short)xp[e][i]); lds_st<unsigned short>(a + 32, (unsigned short)(xp[e][i] >> 16));
      }
#pragma unroll
    for (int j = 0; j < 3; ++j) x1r[j] = lds_ld<u32x4>(L_XS + w * XSP + wv + j * 1024);
  }
  {
    const u32 tl = 16 * (2 * half + (w & 1) + 4 * (w >> 1)) + l15;
    const u32 ntp = tl >> 3, row = 32 * BLK_X(m2, ntp) + BLK_R(m2, ntp) + 256 * (tl & 7);
    const rsrc_t rO = __builtin_amdgcn_make_buffer_rsrc(pout, 0, 2048 * 640 * 2, 0x00020000);
    const u32 fl2 = BLK_X(m2, l15) + 8 * BLK_R(m2, l15);
    u32 cap = fail ? 0 : SPIN_CAP;
    asm volatile(
      "s_lshl_b32 s64, %[w], 11\n\t"
      "s_mul_i32 s66, %[w], 6144\n\t"
      "s_add_u32 s66, s66, %[hown]\n\t"
      "s_add_u32 s68, s66, 4096\n\t"
      "v_mov_b32 v52, %[flo2]\n\t"
      "v_add_u32 v42, %[db], %[wv]\n\t"
      "v_mov_b32 v33, %[ad]\n\t"
      "v_xor_b32 v36, 64, %[ad]\n\t"
      "v_add_u32 v39, s64, %[wv]\n\t"
      "v_add_u32 v34, 0x10000, v33\n\t"
      "v_add_u32 v37, 0x10000, v36\n\t"
      "v_add_u32 v40, 0x10000, v39\n\t"
      "v_add_u32 v35, 0x20000, v33\n\t"
      "v_add_u32 v38, 0x20000, v36\n\t"
      "v_add_u32 v41, 0x20000, v39\n\t"
      "s_and_b32 s63, %[w], 1\n\t"
      "s_mul_i32 s62, s63, 3072\n\t"
      "v_add_u32 v43, s62, %[wv]\n\t"
      "s_sub_u32 s62, 3072, s62\n\t"
      "v_add_u32 v44, s62, %[wv]\n\t"
      "s_mul_i32 s62, s63, 20480\n\t"
      "s_mul_i32 s65, %[w], 5120\n\t"
      "s_add_u32 s60, s62, s65\n\t"
      "v_add_u32 v47, s60, %[wv]\n\t"
      "s_sub_u32 s62, 20480, s62\n\t"
      "s_add_u32 s60, s62, s65\n\t"
      "v_add_u32 v48, s60, %[wv]\n\t"
      "v_lshrrev_b32 v50, 8, %[wv]\n\t"
      "v_lshlrev_b32 v50, 3, v50\n\t"
      "v_sub_u32 v46, %[oo], v50\n\t"
      "v_lshlrev_b32 v50, 1, v50\n\t"
      "v_sub_u32 v32, %[gbo], v50\n\t"
      "v_and_b32 v50, 512, %[wv]\n\t"
      "v_lshlrev_b32 v50, 21, v50\n\t"
      "v_add_u32 v45, v50, %[wv]\n\t"
      "v_add_u32 v45, 328448, v45\n\t"
      "v_mov_b32 v53, %[wv]\n\t"
      "v_mov_b32 v54, %[xw1]\n\t"
      "v_cmp_ne_u32 vcc, 0, %[wv]\n\t"
      "s_nop 1\n\t"
      "v_cndmask_b32 v51, 0, -1, vcc\n\t"
      "v_lshrrev_b32 v51, 1, v51\n\t"
      "v_and_b32 v51, 0x7ff00000, v51\n\t"
      "v_mov_b32 v56, -1\n\t"
      "v_mov_b32 v57, -1\n\t"
      "v_mov_b32 v58, -1\n\t"
      "v_mov_b32 v59, -1\n\t"
      "s_waitcnt lgkmcnt(0)\n\t"
      "s_barrier\n\t"
      "s_sleep " STR(DLY3) "\n\t"
      "s_add_u32 m0, s64, 0\n\t"
      "s_nop 0\n\t"
      "buffer_load_dwordx4 v42, %[rs], m0 offen sc1 lds\n\t"
      "buffer_load_dwordx4 v42, %[rs], m0 offen offset:1024 sc1 lds\n\t"
      "s_add_u32 m0, s64, 16384\n\t"
      "s_nop 0\n\t"
      "buffer_load_dwordx4 v42, %[rs], m0 offen sc1 lds\n\t"
      "buffer_load_dwordx4 v42, %[rs], m0 offen offset:1024 sc1 lds\n\t"
      "s_add_u32 m0, s64, 32768\n\t"
      "s_nop 0\n\t"
      "buffer_load_dwordx4 v42, %[rs], m0 offen sc1 lds\n\t"
      "buffer_load_dwordx4 v42, %[rs], m0 offen offset:1024 sc1 lds\n\t"
      "s_add_u32 m0, s64, 49152\n\t"
      "s_nop 0\n\t"
      "buffer_load_dwordx4 v42, %[rs], m0 offen sc1 lds\n\t"
      "buffer_load_dwordx4 v42, %[rs], m0 offen offset:1024 sc1 lds\n\t"
      "s_add_u32 m0, s64, 65536\n\t"
      "s_nop 0\n\t"
      "buffer_load_dwordx4 v42, %[rs], m0 offen sc1 lds\n\t"
      "buffer_load_dwordx4 v42, %[rs], m0 offen offset:1024 sc1 lds\n\t"
      "s_add_u32 m0, s64, 81920\n\t"
      "s_nop 0\n\t"
      "buffer_load_dwordx4 v42, %[rs], m0 offen sc1 lds\n\t"
      "buffer_load_dwordx4 v42, %[rs], m0 offen offset:1024 sc1 lds\n\t"
      "s_add_u32 m0, s64, 98304\n\t"
      "s_nop 0\n\t"
      "buffer_load_dwordx4 v42, %[rs], m0 offen sc1 lds\n\t"
      "buffer_load_dwordx4 v42, %[rs], m0 offen offset:1024 sc1 lds\n\t"
      "s_add_u32 m0, s64, 114688\n\t"
      "s_nop 0\n\t"
      "buffer_load_dwordx4 v42, %[rs], m0 offen sc1 lds\n\t"
      "buffer_load_dwordx4 v42, %[rs], m0 offen offset:1024 sc1 lds\n\t"
      "s_add_u32 m0, s64, 131072\n\t"
      "s_nop 0\n\t"
      "buffer_load_dwordx4 v42, %[rs], m0 offen sc1 lds\n\t"
      "buffer_load_dwordx4 v42, %[rs], m0 offen offset:1024 sc1 lds\n\t"
      "s_add_u32 m0, s64, 147456\n\t"
      "s_nop 0\n\t"
      "buffer_load_dwordx4 v42, %[rs], m0 offen sc1 lds\n\t"
      "buffer_load_dwordx4 v42, %[rs], m0 offen offset:1024 sc1 lds\n\t"
      "s_waitcnt vmcnt(16)\n\t"
      "ds_read_b128 v[112:115], v39 offset:0\n\t"
      "ds_read_b128 v[116:119], v39 offset:1024\n\t"
      "ds_read_b128 v[120:123], v39 offset:16384\n\t"
      "ds_read_b128 v[124:127], v39 offset:17408\n\t"
      "s_waitcnt vmcnt(12)\n\t"
      "ds_read_b128 v[128:131], v39 offset:32768\n\t"
      "ds_read_b128 v[132:135], v39 offset:33792\n\t"
      "ds_read_b128 v[136:139], v39 offset:49152\n\t"
      "ds_read_b128 v[140:143], v39 offset:50176\n\t"
      "s_waitcnt lgkmcnt(7)\n\t"
      "v_max3_u32 v49, v115, v112, v113\n\t"
      "v_max_u32 v49, v49, v114\n\t"
      "s_waitcnt lgkmcnt(6)\n\t"
      "v_max3_u32 v49, v49, v116, v117\n\t"
      "v_max3_u32 v49, v49, v118, v119\n\t"
      "s_waitcnt lgkmcnt(5)\n\t"
      "v_max3_u32 v49, v49, v120, v121\n\t"
      "v_max3_u32 v49, v49, v122, v123\n\t"
      "s_waitcnt lgkmcnt(4)\n\t"
      "v_max3_u32 v49, v49, v124, v125\n\t"
      "v_max3_u32 v49, v49, v126, v127\n\t"
      "s_waitcnt lgkmcnt(3)\n\t"
      "v_max3_u32 v49, v49, v128, v129\n\t"
      "v_max3_u32 v49, v49, v130, v131\n\t"
      "s_waitcnt lgkmcnt(2)\n\t"
      "v_max3_u32 v49, v49, v132, v133\n\t"
      "v_max3_u32 v49, v49, v134, v135\n\t"
      "s_waitcnt lgkmcnt(1)\n\t"
      "v_max3_u32 v49, v49, v136, v137\n\t"
      "v_max3_u32 v49, v49, v138, v139\n\t"
      "s_waitcnt lgkmcnt(0)\n\t"
      "v_max3_u32 v49, v49, v140, v141\n\t"
      "v_max3_u32 v49, v49, v142, v143\n\t"
      "v_cmp_eq_u32 vcc, -1, v49\n\t"
      "s_cbranch_vccnz .Ls1_g0_%=\n\t"
      ".Lr1_g0_%=:\n\t"
      "s_barrier\n\t"
      "ds_read_b128 v[160:163], v33 offset:0\n\t"
      "ds_read_b128 v[164:167], v33 offset:2048\n\t"
      "ds_read_b128 v[168:171], v33 offset:4096\n\t"
      "ds_read_b128 v[172:175], v33 offset:6144\n\t"
      "ds_read_b128 v[176:179], v36 offset:0\n\t"
      "ds_read_b128 v[180:183], v36 offset:2048\n\t"
      "ds_read_b128 v[184:187], v36 offset:4096\n\t"
      "s_waitcnt lgkmcnt(5)\n\t"
      "v_mfma_f32_16x16x32_bf16 v[64:67], a[0:3], v[160:163], 0\n\t"
      "ds_read_b128 v[188:191], v36 offset:6144\n\t"
      "s_add_u32 m0, s64, 8192\n\t"
      "v_mfma_f32_16x16x32_bf16 v[68:71], a[4:7], v[160:163], 0\n\t"
      "v_mov_b32 v50, %[tag]\n\t"
      "buffer_load_dwordx4 v42, %[rs], m0 offen sc1 lds\n\t"
      "v_mfma_f32_16x16x32_bf16 v[72:75], a[8:11], v[160:163], 0\n\t"
      "v_mfma_f32_16x16x32_bf16 v[76:79], a[0:3], v[164:167], 0\n\t"
      "ds_read_b128 v[160:163], v33 offset:16384\n\t"
      "v_mfma_f32_16x16x32_bf16 v[80:83], a[4:7], v[164:167], 0\n\t"
      "v_mfma_f32_16x16x32_bf16 v[84:87], a[8:11], v[164:167], 0\n\t"
      "buffer_load_dwordx4 v42, %[rs], m0 offen offset:1024 sc1 lds\n\t"
      "s_waitcnt lgkmcnt(5)\n\t"
      "v_mfma_f32_16x16x32_bf16 v[88:91], a[0:3], v[168:171], 0\n\t"
      "ds_read_b128 v[164:167], v33 offset:18432\n\t"
      "v_mfma_f32_16x16x32_bf16 v[92:95], a[4:7], v[168:171], 0\n\t"
      "v_mfma_f32_16x16x32_bf16 v[96:99], a[8:11], v[168:171], 0\n\t"
      "v_mfma_f32_16x16x32_bf16 v[100:103], a[0:3], v[172:175], 0\n\t"
      "ds_read_b128 v[168:171], v33 offset:20480\n\t"
      "v_mfma_f32_16x16x32_bf16 v[104:107], a[4:7], v[172:175], 0\n\t"
      "buffer_store_dwordx4 v[56:59], %[wv], %[rs], s66 offen offset:0 sc1\n\t"
      "v_mfma_f32_16x16x32_bf16 v[108:111], a[8:11], v[172:175], 0\n\t"
      "s_waitcnt lgkmcnt(5)\n\t"
      "v_mfma_f32_16x16x32_bf16 v[64:67], a[12:15], v[176:179], v[64:67]\n\t"
      "ds_read_b128 v[172:175], v33 offset:22528\n\t"
      "v_mfma_f32_16x16x32_bf16 v[68:71], a[16:19], v[176:179], v[68:71]\n\t"
      "v_mfma_f32_16x16x32_bf16 v[72:75], a[20:23], v[176:179], v[72:75]\n\t"
      "s_add_u32 m0, s64, 24576\n\t"
      "v_mfma_f32_16x16x32_bf16 v[76:79], a[12:15], v[180:183], v[76:79]\n\t"
      "ds_read_b128 v[176:179], v36 offset:16384\n\t"
      "buffer_load_dwordx4 v42, %[rs], m0 offen sc1 lds\n\t"
      "v_mfma_f32_16x16x32_bf16 v[80:83], a[16:19], v[180:183], v[80:83]\n\t"
      "v_mfma_f32_16x16x32_bf16 v[84:87], a[20:23], v[180:183], v[84:87]\n\t"
      "s_waitcnt lgkmcnt(5)\n\t"
      "v_mfma_f32_16x16x32_bf16 v[88:91], a[12:15], v[184:187], v[88:91]\n\t"
      "ds_read_b128 v[180:183], v36 offset:18432\n\t"
      "v_mfma_f32_16x16x32_bf16 v[92:95], a[16:19], v[184:187], v[92:95]\n\t"
      "v_mfma_f32_16x16x32_bf16 v[96:99], a[20:23], v[184:187], v[96:99]\n\t"
      "buffer_store_dwordx4 v[56:59], %[wv], %[rs], s66 offen offset:1024 sc1\n\t"
      "v_mfma_f32_16x16x32_bf16 v[100:103], a[12:15], v[188:191], v[100:103]\n\t"
      "ds_read_b128 v[184:187], v36 offset:20480\n\t"
      "v_mfma_f32_16x16x32_bf16 v[104:107], a[16:19], v[188:191], v[104:107]\n\t"
      "v_mfma_f32_16x16x32_bf16 v[108:111], a[20:23], v[188:191], v[108:111]\n\t"
      "s_waitcnt lgkmcnt(5)\n\t"
      "v_mfma_f32_16x16x32_bf16 v[64:67], a[24:27], v[160:163], v[64:67]\n\t"
      "ds_read_b128 v[188:191], v36 offset:22528\n\t"
      "v_mfma_f32_16x16x32_bf16 v[68:71], a[28:31], v[160:163], v[68:71]\n\t"
      "buffer_load_dwordx4 v42, %[rs], m0 offen offset:1024 sc1 lds\n\t"
      "v_mfma_f32_16x16x32_bf16 v[72:75], a[32:35], v[160:163], v[72:75]\n\t"
      "v_mfma_f32_16x16x32_bf16 v[76:79], a[24:27], v[164:167], v[76:79]\n\t"
      "ds_read_b128 v[160:163], v33 offset:32768\n\t"
      "v_mfma_f32_16x16x32_bf16 v[80:83], a[28:31], v[164:167], v[80:83]\n\t"
      "v_mfma_f32_16x16x32_bf16 v[84:87], a[32:35], v[164:167], v[84:87]\n\t"
      "s_add_u32 m0, s64, 40960\n\t"
      "s_waitcnt lgkmcnt(5)\n\t"
      "v_mfma_f32_16x16x32_bf16 v[88:91], a[24:27], v[168:171], v[88:91]\n\t"
      "ds_read_b128 v[164:167], v33 offset:34816\n\t"
      "buffer_load_dwordx4 v42, %[rs], m0 offen sc1 lds\n\t"
      "v_mfma_f32_16x16x32_bf16 v[92:95], a[28:31], v[168:171], v[92:95]\n\t"
      "s_waitcnt vmcnt(7)\n\t"
      "ds_read_b128 v[112:115], v40 offset:0\n\t"
      "ds_read_b128 v[116:119], v40 offset:1024\n\t"
      "v_mfma_f32_16x16x32_bf16 v[96:99], a[32:35], v[168:171], v[96:99]\n\t"
      "ds_read_b128 v[120:123], v40 offset:16384\n\t"
      "ds_read_b128 v[124:127], v40 offset:17408\n\t"
      "v_mfma_f32_16x16x32_bf16 v[100:103], a[24:27], v[172:175], v[100:103]\n\t"
      "ds_read_b128 v[168:171], v33 offset:36864\n\t"
      "ds_read_b128 v[128:131], v40 offset:32768\n\t"
      "v_mfma_f32_16x16x32_bf16 v[104:107], a[28:31], v[172:175], v[104:107]\n\t"
      "ds_read_b128 v[132:135], v40 offset:33792\n\t"
      "ds_read_b128 v[136:139], v40 offset:49152\n\t"
      "v_mfma_f32_16x16x32_bf16 v[108:111], a[32:35], v[172:175], v[108:111]\n\t"
      "ds_read_b128 v[140:143], v40 offset:50176\n\t"
      "s_waitcnt lgkmcnt(13)\n\t"
      "v_mfma_f32_16x16x32_bf16 v[64:67], a[36:39], v[176:179], v[64:67]\n\t"
      "ds_read_b128 v[172:175], v33 offset:38912\n\t"
      "ds_read_b128 v[144:147], v41 offset:0\n\t"
      "v_mfma_f32_16x16x32_bf16 v[68:71], a[40:43], v[176:179], v[68:71]\n\t"
      "ds_read_b128 v[148:151], v41 offset:1024\n\t"
      "ds_read_b128 v[152:155], v41 offset:16384\n\t"
      "v_mfma_f32_16x16x32_bf16 v[72:75], a[44:47], v[176:179], v[72:75]\n\t"
      "ds_read_b128 v[156:159], v41 offset:17408\n\t"
      "buffer_store_dwordx4 v[56:59], %[wv], %[rs], s66 offen offset:2048 sc1\n\t"
      "v_mfma_f32_16x16x32_bf16 v[76:79], a[36:39], v[180:183], v[76:79]\n\t"
      "ds_read_b128 v[176:179], v36 offset:32768\n\t"
      "v_mfma_f32_16x16x32_bf16 v[80:83], a[40:43], v[180:183], v[80:83]\n\t"
      "buffer_load_dwordx4 v42, %[rs], m0 offen offset:1024 sc1 lds\n\t"
      "v_mfma_f32_16x16x32_bf16 v[84:87], a[44:47], v[180:183], v[84:87]\n\t"
      "s_waitcnt lgkmcnt(14)\n\t"
      "v_max3_u32 v49, v115, v112, v113\n\t"
      "v_mfma_f32_16x16x32_bf16 v[88:91], a[36:39], v[184:187], v[88:91]\n\t"
      "ds_read_b128 v[180:183], v36 offset:34816\n\t"
      "v_max_u32 v49, v49, v114\n\t"
      "v_mfma_f32_16x16x32_bf16 v[92:95], a[40:43], v[184:187], v[92:95]\n\t"
      "s_waitcnt lgkmcnt(14)\n\t"
      "v_max3_u32 v49, v49, v116, v117\n\t"
      "v_max3_u32 v49, v49, v118, v119\n\t"
      "v_mfma_f32_16x16x32_bf16 v[96:99], a[44:47], v[184:187], v[96:99]\n\t"
      "s_waitcnt lgkmcnt(13)\n\t"
      "v_max3_u32 v49, v49, v120, v121\n\t"
      "v_max3_u32 v49, v49, v122, v123\n\t"
      "v_mfma_f32_16x16x32_bf16 v[100:103], a[36:39], v[188:191], v[100:103]\n\t"
      "ds_read_b128 v[184:187], v36 offset:36864\n\t"
      "s_waitcnt lgkmcnt(13)\n\t"
      "v_max3_u32 v49, v49, v124, v125\n\t"
      "v_mfma_f32_16x16x32_bf16 v[104:107], a[40:43], v[188:191], v[104:107]\n\t"
      "v_max3_u32 v49, v49, v126, v127\n\t"
      "s_waitcnt lgkmcnt(11)\n\t"
      "v_max3_u32 v49, v49, v128, v129\n\t"
      "v_mfma_f32_16x16x32_bf16 v[108:111], a[44:47], v[188:191], v[108:111]\n\t"
      "v_max3_u32 v49, v49, v130, v131\n\t"
      "v_mfma_f32_16x16x32_bf16 v[64:67], a[48:51], v[160:163], v[64:67]\n\t"
      "ds_read_b128 v[188:191], v36 offset:38912\n\t"
      "s_waitcnt lgkmcnt(11)\n\t"
      "v_max3_u32 v49, v49, v132, v133\n\t"
      "v_mfma_f32_16x16x32_bf16 v[68:71], a[52:55], v[160:163], v[68:71]\n\t"
      "v_max3_u32 v49, v49, v134, v135\n\t"
      "s_waitcnt lgkmcnt(10)\n\t"
      "v_max3_u32 v49, v49, v136, v137\n\t"
      "v_mfma_f32_16x16x32_bf16 v[72:75], a[56:59], v[160:163], v[72:75]\n\t"
      "v_max3_u32 v49, v49, v138, v139\n\t"
      "s_waitcnt lgkmcnt(9)\n\t"
      "v_max3_u32 v49, v49, v140, v141\n\t"
      "v_mfma_f32_16x16x32_bf16 v[76:79], a[48:51], v[164:167], v[76:79]\n\t"
      "ds_read_b128 v[160:163], v33 offset:49152\n\t"
      "v_max3_u32 v49, v49, v142, v143\n\t"
      "v_mfma_f32_16x16x32_bf16 v[80:83], a[52:55], v[164:167], v[80:83]\n\t"
      "s_waitcnt lgkmcnt(8)\n\t"
      "v_max3_u32 v49, v49, v144, v145\n\t"
      "v_max3_u32 v49, v49, v146, v147\n\t"
      "v_mfma_f32_16x16x32_bf16 v[84:87], a[56:59], v[164:167], v[84:87]\n\t"
      "s_waitcnt lgkmcnt(7)\n\t"
      "v_max3_u32 v49, v49, v148, v149\n\t"
      "v_mfma_f32_16x16x32_bf16 v[88:91], a[48:51], v[168:171], v[88:91]\n\t"
      "ds_read_b128 v[164:167], v33 offset:51200\n\t"
      "v_max3_u32 v49, v49, v150, v151\n\t"
      "v_mfma_f32_16x16x32_bf16 v[92:95], a[52:55], v[168:171], v[92:95]\n\t"
      "s_waitcnt lgkmcnt(7)\n\t"
      "v_max3_u32 v49, v49, v152, v153\n\t"
      "v_max3_u32 v49, v49, v154, v155\n\t"
      "v_mfma_f32_16x16x32_bf16 v[96:99], a[56:59], v[168:171], v[96:99]\n\t"
      "s_waitcnt lgkmcnt(6)\n\t"
      "v_max3_u32 v49, v49, v156, v157\n\t"
      "v_max3_u32 v49, v49, v158, v159\n\t"
      "v_mfma_f32_16x16x32_bf16 v[100:103], a[48:51], v[172:175], v[100:103]\n\t"
      "ds_read_b128 v[168:171], v33 offset:53248\n\t"
      "buffer_store_dwordx4 v[56:59], %[wv], %[rs], s66 offen offset:3072 sc1\n\t"
      "v_mfma_f32_16x16x32_bf16 v[104:107], a[52:55], v[172:175], v[104:107]\n\t"
      "buffer_store_dwordx4 v[56:59], %[wv], %[rs], s68 offen offset:0 sc1\n\t"
      "v_mfma_f32_16x16x32_bf16 v[108:111], a[56:59], v[172:175], v[108:111]\n\t"
      "s_waitcnt lgkmcnt(5)\n\t"
      "v_mfma_f32_16x16x32_bf16 v[64:67], a[60:63], v[176:179], v[64:67]\n\t"
      "ds_read_b128 v[172:175], v33 offset:55296\n\t"
      "v_mfma_f32_16x16x32_bf16 v[68:71], a[64:67], v[176:179], v[68:71]\n\t"
      "v_mfma_f32_16x16x32_bf16 v[72:75], a[68:71], v[176:179], v[72:75]\n\t"
      "s_add_u32 m0, s64, 57344\n\t"
      "v_mfma_f32_16x16x32_bf16 v[76:79], a[60:63], v[180:183], v[76:79]\n\t"
      "ds_read_b128 v[176:179], v36 offset:49152\n\t"
      "buffer_load_dwordx4 v42, %[rs], m0 offen sc1 lds\n\t"
      "v_mfma_f32_16x16x32_bf16 v[80:83], a[64:67], v[180:183], v[80:83]\n\t"
      "v_mfma_f32_16x16x32_bf16 v[84:87], a[68:71], v[180:183], v[84:87]\n\t"
      "s_waitcnt lgkmcnt(5)\n\t"
      "v_mfma_f32_16x16x32_bf16 v[88:91], a[60:63], v[184:187], v[88:91]\n\t"
      "ds_read_b128 v[180:183], v36 offset:51200\n\t"
      "v_mfma_f32_16x16x32_bf16 v[92:95], a[64:67], v[184:187], v[92:95]\n\t"
      "v_mfma_f32_16x16x32_bf16 v[96:99], a[68:71], v[184:187], v[96:99]\n\t"
      "buffer_store_dwordx4 v[56:59], %[wv], %[rs], s68 offen offset:1024 sc1\n\t"
      "v_mfma_f32_16x16x32_bf16 v[100:103], a[60:63], v[188:191], v[100:103]\n\t"
      "ds_read_b128 v[184:187], v36 offset:53248\n\t"
      "v_mfma_f32_16x16x32_bf16 v[104:107], a[64:67], v[188:191], v[104:107]\n\t"
      "v_mfma_f32_16x16x32_bf16 v[108:111], a[68:71], v[188:191], v[108:111]\n\t"
      "s_waitcnt lgkmcnt(5)\n\t"
      "v_mfma_f32_16x16x32_bf16 v[64:67], a[72:75], v[160:163], v[64:67]\n\t"
      "ds_read_b128 v[188:191], v36 offset:55296\n\t"
      "v_mfma_f32_16x16x32_bf16 v[68:71], a[76:79], v[160:163], v[68:71]\n\t"
      "v_cmp_eq_u32 vcc, -1, v49\n\t"
      "buffer_load_dwordx4 v42, %[rs], m0 offen offset:1024 sc1 lds\n\t"
      "v_mfma_f32_16x16x32_bf16 v[72:75], a[80:83], v[160:163], v[72:75]\n\t"
      "s_cbranch_vccnz .Ls1_g2_%=\n\t"
      ".Lr1_g2_%=:\n\t"
      "s_barrier\n\t"
      "v_mfma_f32_16x16x32_bf16 v[76:79], a[72:75], v[164:167], v[76:79]\n\t"
      "ds_read_b128 v[160:163], v34 offset:0\n\t"
      "v_mfma_f32_16x16x32_bf16 v[80:83], a[76:79], v[164:167], v[80:83]\n\t"
      "v_mfma_f32_16x16x32_bf16 v[84:87], a[80:83], v[164:167], v[84:87]\n\t"
      "s_add_u32 m0, s64, 73728\n\t"
      "s_waitcnt lgkmcnt(5)\n\t"
      "v_mfma_f32_16x16x32_bf16 v[88:91], a[72:75], v[168:171], v[88:91]\n\t"
      "ds_read_b128 v[164:167], v34 offset:2048\n\t"
      "buffer_load_dwordx4 v42, %[rs], m0 offen sc1 lds\n\t"
      "v_mfma_f32_16x16x32_bf16 v[92:95], a[76:79], v[168:171], v[92:95]\n\t"
      "v_mfma_f32_16x16x32_bf16 v[96:99], a[80:83], v[168:171], v[96:99]\n\t"
      "v_mfma_f32_16x16x32_bf16 v[100:103], a[72:75], v[172:175], v[100:103]\n\t"
      "ds_read_b128 v[168:171], v34 offset:4096\n\t"
      "v_mfma_f32_16x16x32_bf16 v[104:107], a[76:79], v[172:175], v[104:107]\n\t"
      "v_mfma_f32_16x16x32_bf16 v[108:111], a[80:83], v[172:175], v[108:111]\n\t"
      "buffer_store_dwordx4 %[x0], v53, %[ro], %[x1o] offen sc1\n\t"
      "s_waitcnt lgkmcnt(5)\n\t"
      "v_mfma_f32_16x16x32_bf16 v[64:67], a[84:87], v[176:179], v[64:67]\n\t"
      "ds_read_b128 v[172:175], v34 offset:6144\n\t"
      "v_mfma_f32_16x16x32_bf16 v[68:71], a[88:91], v[176:179], v[68:71]\n\t"
      "v_mfma_f32_16x16x32_bf16 v[72:75], a[92:95], v[176:179], v[72:75]\n\t"
      "v_mfma_f32_16x16x32_bf16 v[76:79], a[84:87], v[180:183], v[76:79]\n\t"
      "ds_read_b128 v[176:179], v37 offset:0\n\t"
      "v_mfma_f32_16x16x32_bf16 v[80:83], a[88:91], v[180:183], v[80:83]\n\t"
      "buffer_load_dwordx4 v42, %[rs], m0 offen offset:1024 sc1 lds\n\t"
      "v_mfma_f32_16x16x32_bf16 v[84:87], a[92:95], v[180:183], v[84:87]\n\t"
      "s_waitcnt lgkmcnt(5)\n\t"
      "v_mfma_f32_16x16x32_bf16 v[88:91], a[84:87], v[184:187], v[88:91]\n\t"
      "ds_read_b128 v[180:183], v37 offset:2048\n\t"
      "v_mfma_f32_16x16x32_bf16 v[92:95], a[88:91], v[184:187], v[92:95]\n\t"
      "v_mfma_f32_16x16x32_bf16 v[96:99], a[92:95], v[184:187], v[96:99]\n\t"
      "v_mfma_f32_16x16x32_bf16 v[100:103], a[84:87], v[188:191], v[100:103]\n\t"
      "ds_read_b128 v[184:187], v37 offset:4096\n\t"
      "buffer_store_dwordx4 %[x1], v54, %[ro], %[x1o] offen sc1\n\t"
      "v_mfma_f32_16x16x32_bf16 v[104:107], a[88:91], v[188:191], v[104:107]\n\t"
      "v_mfma_f32_16x16x32_bf16 v[108:111], a[92:95], v[188:191], v[108:111]\n\t"
      "s_waitcnt lgkmcnt(5)\n\t"
      "v_mfma_f32_16x16x32_bf16 v[64:67], a[96:99], v[160:163], v[64:67]\n\t"
      "ds_read_b128 v[188:191], v37 offset:6144\n\t"
      "v_mfma_f32_16x16x32_bf16 v[68:71], a[100:103], v[160:163], v[68:71]\n\t"
      "s_add_u32 m0, s64, 90112\n\t"
      "v_mfma_f32_16x16x32_bf16 v[72:75], a[104:107], v[160:163], v[72:75]\n\t"
      "buffer_load_dwordx4 v42, %[rs], m0 offen sc1 lds\n\t"
      "v_mfma_f32_16x16x32_bf16 v[76:79], a[96:99], v[164:167], v[76:79]\n\t"
      "ds_read_b128 v[160:163], v34 offset:16384\n\t"
      "v_mfma_f32_16x16x32_bf16 v[80:83], a[100:103], v[164:167], v[80:83]\n\t"
      "v_mfma_f32_16x16x32_bf16 v[84:87], a[104:107], v[164:167], v[84:87]\n\t"
      "s_waitcnt lgkmcnt(5)\n\t"
      "v_mfma_f32_16x16x32_bf16 v[88:91], a[96:99], v[168:171], v[88:91]\n\t"
      "ds_read_b128 v[164:167], v34 offset:18432\n\t"
      "v_mfma_f32_16x16x32_bf16 v[92:95], a[100:103], v[168:171], v[92:95]\n\t"
      "buffer_load_dwordx4 v42, %[rs], m0 offen offset:1024 sc1 lds\n\t"
      "v_mfma_f32_16x16x32_bf16 v[96:99], a[104:107], v[168:171], v[96:99]\n\t"
      "v_mfma_f32_16x16x32_bf16 v[100:103], a[96:99], v[172:175], v[100:103]\n\t"
      "ds_read_b128 v[168:171], v34 offset:20480\n\t"
      "v_mfma_f32_16x16x32_bf16 v[104:107], a[100:103], v[172:175], v[104:107]\n\t"
      "v_mfma_f32_16x16x32_bf16 v[108:111], a[104:107], v[172:175], v[108:111]\n\t"
      "s_waitcnt lgkmcnt(5)\n\t"
      "v_mfma_f32_16x16x32_bf16 v[64:67], a[108:111], v[176:179], v[64:67]\n\t"
      "ds_read_b128 v[172:175], v34 offset:22528\n\t"
      "buffer_store_dwordx4 %[x2], v45, %[ro], %[x1o] offen sc1\n\t"
      "v_mfma_f32_16x16x32_bf16 v[68:71], a[112:115], v[176:179], v[68:71]\n\t"
      "v_mfma_f32_16x16x32_bf16 v[72:75], a[116:119], v[176:179], v[72:75]\n\t"
      "v_mfma_f32_16x16x32_bf16 v[76:79], a[108:111], v[180:183], v[76:79]\n\t"
      "ds_read_b128 v[176:179], v37 offset:16384\n\t"
      "v_mfma_f32_16x16x32_bf16 v[80:83], a[112:115], v[180:183], v[80:83]\n\t"
      "s_add_u32 s61, %[w2b], 0\n\t"
      "v_mfma_f32_16x16x32_bf16 v[84:87], a[116:119], v[180:183], v[84:87]\n\t"
      "buffer_load_dwordx4 v[208:211], %[wv], %[rs], s61 offen offset:0 sc1\n\t"
      "s_waitcnt lgkmcnt(5)\n\t"
      "v_mfma_f32_16x16x32_bf16 v[88:91], a[108:111], v[184:187], v[88:91]\n\t"
      "ds_read_b128 v[180:183], v37 offset:18432\n\t"
      "v_mfma_f32_16x16x32_bf16 v[92:95], a[112:115], v[184:187], v[92:95]\n\t"
      "v_mfma_f32_16x16x32_bf16 v[96:99], a[116:119], v[184:187], v[96:99]\n\t"
      "v_mfma_f32_16x16x32_bf16 v[100:103], a[108:111], v[188:191], v[100:103]\n\t"
      "ds_read_b128 v[184:187], v37 offset:20480\n\t"
      "v_mfma_f32_16x16x32_bf16 v[104:107], a[112:115], v[188:191], v[104:107]\n\t"
      "buffer_load_dwordx4 v[212:215], %[wv], %[rs], s61 offen offset:1024 sc1\n\t"
      "v_mfma_f32_16x16x32_bf16 v[108:111], a[116:119], v[188:191], v[108:111]\n\t"
      "s_waitcnt lgkmcnt(5)\n\t"
      "v_mfma_f32_16x16x32_bf16 v[64:67], a[120:123], v[160:163], v[64:67]\n\t"
      "ds_read_b128 v[188:191], v37 offset:22528\n\t"
      "v_mfma_f32_16x16x32_bf16 v[68:71], a[124:127], v[160:163], v[68:71]\n\t"
      "v_mfma_f32_16x16x32_bf16 v[72:75], a[128:131], v[160:163], v[72:75]\n\t"
      "v_mfma_f32_16x16x32_bf16 v[76:79], a[120:123], v[164:167], v[76:79]\n\t"
      "ds_read_b128 v[160:163], v34 offset:32768\n\t"
      "buffer_load_dwordx4 v[216:219], %[wv], %[rs], s61 offen offset:2048 sc1\n\t"
      "v_mfma_f32_16x16x32_bf16 v[80:83], a[124:127], v[164:167], v[80:83]\n\t"
      "v_mfma_f32_16x16x32_bf16 v[84:87], a[128:131], v[164:167], v[84:87]\n\t"
      "s_waitcnt lgkmcnt(5)\n\t"
      "v_mfma_f32_16x16x32_bf16 v[88:91], a[120:123], v[168:171], v[88:91]\n\t"
      "ds_read_b128 v[164:167], v34 offset:34816\n\t"
      "v_mfma_f32_16x16x32_bf16 v[92:95], a[124:127], v[168:171], v[92:95]\n\t"
      "v_mfma_f32_16x16x32_bf16 v[96:99], a[128:131], v[168:171], v[96:99]\n\t"
      "buffer_load_dwordx4 v[220:223], %[wv], %[rs], s61 offen offset:3072 sc1\n\t"
      "v_mfma_f32_16x16x32_bf16 v[100:103], a[120:123], v[172:175], v[100:103]\n\t"
      "ds_read_b128 v[168:171], v34 offset:36864\n\t"
      "v_mfma_f32_16x16x32_bf16 v[104:107], a[124:127], v[172:175], v[104:107]\n\t"
      "v_mfma_f32_16x16x32_bf16 v[108:111], a[128:131], v[172:175], v[108:111]\n\t"
      "s_waitcnt lgkmcnt(5)\n\t"
      "v_mfma_f32_16x16x32_bf16 v[64:67], a[132:135], v[176:179], v[64:67]\n\t"
      "ds_read_b128 v[172:175], v34 offset:38912\n\t"
      "s_add_u32 s61, %[w2b], 4096\n\t"
      "v_mfma_f32_16x16x32_bf16 v[68:71], a[136:139], v[176:179], v[68:71]\n\t"
      "buffer_load_dwordx4 v[224:227], %[wv], %[rs], s61 offen offset:0 sc1\n\t"
      "v_mfma_f32_16x16x32_bf16 v[72:75], a[140:143], v[176:179], v[72:75]\n\t"
      "v_mfma_f32_16x16x32_bf16 v[76:79], a[132:135], v[180:183], v[76:79]\n\t"
      "ds_read_b128 v[176:179], v37 offset:32768\n\t"
      "v_mfma_f32_16x16x32_bf16 v[80:83], a[136:139], v[180:183], v[80:83]\n\t"
      "v_mfma_f32_16x16x32_bf16 v[84:87], a[140:143], v[180:183], v[84:87]\n\t"
      "s_waitcnt lgkmcnt(5)\n\t"
      "v_mfma_f32_16x16x32_bf16 v[88:91], a[132:135], v[184:187], v[88:91]\n\t"
      "ds_read_b128 v[180:183], v37 offset:34816\n\t"
      "buffer_load_dwordx4 v[228:231], %[wv], %[rs], s61 offen offset:1024 sc1\n\t"
      "v_mfma_f32_16x16x32_bf16 v[92:95], a[136:139], v[184:187], v[92:95]\n\t"
      "v_mfma_f32_16x16x32_bf16 v[96:99], a[140:143], v[184:187], v[96:99]\n\t"
      "v_mfma_f32_16x16x32_bf16 v[100:103], a[132:135], v[188:191], v[100:103]\n\t"
      "ds_read_b128 v[184:187], v37 offset:36864\n\t"
      "v_mfma_f32_16x16x32_bf16 v[104:107], a[136:139], v[188:191], v[104:107]\n\t"
      "v_mfma_f32_16x16x32_bf16 v[108:111], a[140:143], v[188:191], v[108:111]\n\t"
      "buffer_load_dwordx4 v[232:235], %[wv], %[rs], s61 offen offset:2048 sc1\n\t"
      "s_waitcnt lgkmcnt(5)\n\t"
      "v_mfma_f32_16x16x32_bf16 v[64:67], a[144:147], v[160:163], v[64:67]\n\t"
      "ds_read_b128 v[188:191], v37 offset:38912\n\t"
      "v_mfma_f32_16x16x32_bf16 v[68:71], a[148:151], v[160:163], v[68:71]\n\t"
      "v_mfma_f32_16x16x32_bf16 v[72:75], a[152:155], v[160:163], v[72:75]\n\t"
      "v_mfma_f32_16x16x32_bf16 v[76:79], a[144:147], v[164:167], v[76:79]\n\t"
      "ds_read_b128 v[160:163], v34 offset:49152\n\t"
      "v_mfma_f32_16x16x32_bf16 v[80:83], a[148:151], v[164:167], v[80:83]\n\t"
      "buffer_load_dwordx4 v[236:239], %[wv], %[rs], s61 offen offset:3072 sc1\n\t"
      "v_mfma_f32_16x16x32_bf16 v[84:87], a[152:155], v[164:167], v[84:87]\n\t"
      "s_waitcnt lgkmcnt(5)\n\t"
      "v_mfma_f32_16x16x32_bf16 v[88:91], a[144:147], v[168:171], v[88:91]\n\t"
      "ds_read_b128 v[164:167], v34 offset:51200\n\t"
      "v_mfma_f32_16x16x32_bf16 v[92:95], a[148:151], v[168:171], v[92:95]\n\t"
      "v_mfma_f32_16x16x32_bf16 v[96:99], a[152:155], v[168:171], v[96:99]\n\t"
      "s_add_u32 s61, %[w2b], 8192\n\t"
      "v_mfma_f32_16x16x32_bf16 v[100:103], a[144:147], v[172:175], v[100:103]\n\t"
      "ds_read_b128 v[168:171], v34 offset:53248\n\t"
      "buffer_load_dwordx4 v[240:243], %[wv], %[rs], s61 offen offset:0 sc1\n\t"
      "v_mfma_f32_16x16x32_bf16 v[104:107], a[148:151], v[172:175], v[104:107]\n\t"
      "v_mfma_f32_16x16x32_bf16 v[108:111], a[152:155], v[172:175], v[108:111]\n\t"
      "s_waitcnt lgkmcnt(5)\n\t"
      "v_mfma_f32_16x16x32_bf16 v[64:67], a[156:159], v[176:179], v[64:67]\n\t"
      "ds_read_b128 v[172:175], v34 offset:55296\n\t"
      "v_mfma_f32_16x16x32_bf16 v[68:71], a[160:163], v[176:179], v[68:71]\n\t"
      "v_mfma_f32_16x16x32_bf16 v[72:75], a[164:167], v[176:179], v[72:75]\n\t"
      "buffer_load_dwordx4 v[244:247], %[wv], %[rs], s61 offen offset:1024 sc1\n\t"
      "v_mfma_f32_16x16x32_bf16 v[76:79], a[156:159], v[180:183], v[76:79]\n\t"
      "ds_read_b128 v[176:179], v37 offset:49152\n\t"
      "v_mfma_f32_16x16x32_bf16 v[80:83], a[160:163], v[180:183], v[80:83]\n\t"
      "s_waitcnt vmcnt(10)\n\t"
      "buffer_store_dword v50, v51, %[rs], %[fown] offen offset:16 sc1\n\t"
      "v_mfma_f32_16x16x32_bf16 v[84:87], a[164:167], v[180:183], v[84:87]\n\t"
      "s_waitcnt lgkmcnt(5)\n\t"
      "v_mfma_f32_16x16x32_bf16 v[88:91], a[156:159], v[184:187], v[88:91]\n\t"
      "ds_read_b128 v[180:183], v37 offset:51200\n\t"
      "v_mfma_f32_16x16x32_bf16 v[92:95], a[160:163], v[184:187], v[92:95]\n\t"
      "buffer_load_dwordx4 v[248:251], %[wv], %[rs], s61 offen offset:2048 sc1\n\t"
      "v_mfma_f32_16x16x32_bf16 v[96:99], a[164:167], v[184:187], v[96:99]\n\t"
      "v_mfma_f32_16x16x32_bf16 v[100:103], a[156:159], v[188:191], v[100:103]\n\t"
      "ds_read_b128 v[184:187], v37 offset:53248\n\t"
      "v_mfma_f32_16x16x32_bf16 v[104:107], a[160:163], v[188:191], v[104:107]\n\t"
      "v_mfma_f32_16x16x32_bf16 v[108:111], a[164:167], v[188:191], v[108:111]\n\t"
      "s_waitcnt lgkmcnt(5)\n\t"
      "v_mfma_f32_16x16x32_bf16 v[64:67], a[168:171], v[160:163], v[64:67]\n\t"
      "ds_read_b128 v[188:191], v37 offset:55296\n\t"
      "buffer_load_dwordx4 v[252:255], %[wv], %[rs], s61 offen offset:3072 sc1\n\t"
      "v_mfma_f32_16x16x32_bf16 v[68:71], a[172:175], v[160:163], v[68:71]\n\t"
      "v_mfma_f32_16x16x32_bf16 v[72:75], a[176:179], v[160:163], v[72:75]\n\t"
      "v_mfma_f32_16x16x32_bf16 v[76:79], a[168:171], v[164:167], v[76:79]\n\t"
      "ds_read_b128 v[160:163], v35 offset:0\n\t"
      "v_mfma_f32_16x16x32_bf16 v[80:83], a[172:175], v[164:167], v[80:83]\n\t"
      "s_add_u32 s61, %[w2b], 12288\n\t"
      "v_mfma_f32_16x16x32_bf16 v[84:87], a[176:179], v[164:167], v[84:87]\n\t"
      "buffer_load_dwordx4 a[240:243], %[wv], %[rs], s61 offen offset:0 sc1\n\t"
      "s_waitcnt lgkmcnt(5)\n\t"
      "v_mfma_f32_16x16x32_bf16 v[88:91], a[168:171], v[168:171], v[88:91]\n\t"
      "ds_read_b128 v[164:167], v35 offset:2048\n\t"
      "v_mfma_f32_16x16x32_bf16 v[92:95], a[172:175], v[168:171], v[92:95]\n\t"
      "ds_read_b128 v[112:115], v39 offset:8192\n\t"
      "ds_read_b128 v[116:119], v39 offset:9216\n\t"
      "v_mfma_f32_16x16x32_bf16 v[96:99], a[176:179], v[168:171], v[96:99]\n\t"
      "ds_read_b128 v[120:123], v39 offset:24576\n\t"
      "ds_read_b128 v[124:127], v39 offset:25600\n\t"
      "v_mfma_f32_16x16x32_bf16 v[100:103], a[168:171], v[172:175], v[100:103]\n\t"
      "ds_read_b128 v[168:171], v35 offset:4096\n\t"
      "ds_read_b128 v[128:131], v39 offset:40960\n\t"
      "v_mfma_f32_16x16x32_bf16 v[104:107], a[172:175], v[172:175], v[104:107]\n\t"
      "ds_read_b128 v[132:135], v39 offset:41984\n\t"
      "ds_read_b128 v[136:139], v39 offset:57344\n\t"
      "v_mfma_f32_16x16x32_bf16 v[108:111], a[176:179], v[172:175], v[108:111]\n\t"
      "ds_read_b128 v[140:143], v39 offset:58368\n\t"
      "s_waitcnt lgkmcnt(13)\n\t"
      "v_mfma_f32_16x16x32_bf16 v[64:67], a[180:183], v[176:179], v[64:67]\n\t"
      "ds_read_b128 v[172:175], v35 offset:6144\n\t"
      "ds_read_b128 v[144:147], v40 offset:8192\n\t"
      "v_mfma_f32_16x16x32_bf16 v[68:71], a[184:187], v[176:179], v[68:71]\n\t"
      "ds_read_b128 v[148:151], v40 offset:9216\n\t"
      "ds_read_b128 v[152:155], v40 offset:24576\n\t"
      "v_mfma_f32_16x16x32_bf16 v[72:75], a[188:191], v[176:179], v[72:75]\n\t"
      "ds_read_b128 v[156:159], v40 offset:25600\n\t"
      "buffer_load_dwordx4 a[244:247], %[wv], %[rs], s61 offen offset:1024 sc1\n\t"
      "v_mfma_f32_16x16x32_bf16 v[76:79], a[180:183], v[180:183], v[76:79]\n\t"
      "ds_read_b128 v[176:179], v38 offset:0\n\t"
      "buffer_load_dwordx4 a[248:251], %[wv], %[rs], s61 offen offset:2048 sc1\n\t"
      "v_mfma_f32_16x16x32_bf16 v[80:83], a[184:187], v[180:183], v[80:83]\n\t"
      "v_mfma_f32_16x16x32_bf16 v[84:87], a[188:191], v[180:183], v[84:87]\n\t"
      "s_waitcnt lgkmcnt(14)\n\t"
      "v_max3_u32 v49, v115, v112, v113\n\t"
      "v_mfma_f32_16x16x32_bf16 v[88:91], a[180:183], v[184:187], v[88:91]\n\t"
      "ds_read_b128 v[180:183], v38 offset:2048\n\t"
      "v_max_u32 v49, v49, v114\n\t"
      "v_mfma_f32_16x16x32_bf16 v[92:95], a[184:187], v[184:187], v[92:95]\n\t"
      "s_waitcnt lgkmcnt(14)\n\t"
      "v_max3_u32 v49, v49, v116, v117\n\t"
      "v_max3_u32 v49, v49, v118, v119\n\t"
      "v_mfma_f32_16x16x32_bf16 v[96:99], a[188:191], v[184:187], v[96:99]\n\t"
      "s_waitcnt lgkmcnt(13)\n\t"
      "v_max3_u32 v49, v49, v120, v121\n\t"
      "v_max3_u32 v49, v49, v122, v123\n\t"
      "v_mfma_f32_16x16x32_bf16 v[100:103], a[180:183], v[188:191], v[100:103]\n\t"
      "ds_read_b128 v[184:187], v38 offset:4096\n\t"
      "s_waitcnt lgkmcnt(13)\n\t"
      "v_max3_u32 v49, v49, v124, v125\n\t"
      "v_mfma_f32_16x16x32_bf16 v[104:107], a[184:187], v[188:191], v[104:107]\n\t"
      "v_max3_u32 v49, v49, v126, v127\n\t"
      "s_waitcnt lgkmcnt(11)\n\t"
      "v_max3_u32 v49, v49, v128, v129\n\t"
      "v_mfma_f32_16x16x32_bf16 v[108:111], a[188:191], v[188:191], v[108:111]\n\t"
      "v_max3_u32 v49, v49, v130, v131\n\t"
      "v_mfma_f32_16x16x32_bf16 v[64:67], a[192:195], v[160:163], v[64:67]\n\t"
      "ds_read_b128 v[188:191], v38 offset:6144\n\t"
      "s_waitcnt lgkmcnt(11)\n\t"
      "v_max3_u32 v49, v49, v132, v133\n\t"
      "v_mfma_f32_16x16x32_bf16 v[68:71], a[196:199], v[160:163], v[68:71]\n\t"
      "v_max3_u32 v49, v49, v134, v135\n\t"
      "s_waitcnt lgkmcnt(10)\n\t"
      "v_max3_u32 v49, v49, v136, v137\n\t"
      "v_mfma_f32_16x16x32_bf16 v[72:75], a[200:203], v[160:163], v[72:75]\n\t"
      "v_max3_u32 v49, v49, v138, v139\n\t"
      "s_waitcnt lgkmcnt(9)\n\t"
      "v_max3_u32 v49, v49, v140, v141\n\t"
      "v_mfma_f32_16x16x32_bf16 v[76:79], a[192:195], v[164:167], v[76:79]\n\t"
      "ds_read_b128 v[160:163], v35 offset:16384\n\t"
      "v_max3_u32 v49, v49, v142, v143\n\t"
      "v_mfma_f32_16x16x32_bf16 v[80:83], a[196:199], v[164:167], v[80:83]\n\t"
      "s_waitcnt lgkmcnt(8)\n\t"
      "v_max3_u32 v49, v49, v144, v145\n\t"
      "v_max3_u32 v49, v49, v146, v147\n\t"
      "v_mfma_f32_16x16x32_bf16 v[84:87], a[200:203], v[164:167], v[84:87]\n\t"
      "s_waitcnt lgkmcnt(7)\n\t"
      "v_max3_u32 v49, v49, v148, v149\n\t"
      "v_mfma_f32_16x16x32_bf16 v[88:91], a[192:195], v[168:171], v[88:91]\n\t"
      "ds_read_b128 v[164:167], v35 offset:18432\n\t"
      "v_max3_u32 v49, v49, v150, v151\n\t"
      "v_mfma_f32_16x16x32_bf16 v[92:95], a[196:199], v[168:171], v[92:95]\n\t"
      "s_waitcnt lgkmcnt(7)\n\t"
      "v_max3_u32 v49, v49, v152, v153\n\t"
      "v_max3_u32 v49, v49, v154, v155\n\t"
      "v_mfma_f32_16x16x32_bf16 v[96:99], a[200:203], v[168:171], v[96:99]\n\t"
      "s_waitcnt lgkmcnt(6)\n\t"
      "v_max3_u32 v49, v49, v156, v157\n\t"
      "v_max3_u32 v49, v49, v158, v159\n\t"
      "v_mfma_f32_16x16x32_bf16 v[100:103], a[192:195], v[172:175], v[100:103]\n\t"
      "ds_read_b128 v[168:171], v35 offset:20480\n\t"
      "buffer_load_dwordx4 a[252:255], %[wv], %[rs], s61 offen offset:3072 sc1\n\t"
      "v_mfma_f32_16x16x32_bf16 v[104:107], a[196:199], v[172:175], v[104:107]\n\t"
      "s_add_u32 m0, s64, 106496\n\t"
      "v_mfma_f32_16x16x32_bf16 v[108:111], a[200:203], v[172:175], v[108:111]\n\t"
      "buffer_load_dwordx4 v42, %[rs], m0 offen sc1 lds\n\t"
      "s_waitcnt lgkmcnt(5)\n\t"
      "v_mfma_f32_16x16x32_bf16 v[64:67], a[204:207], v[176:179], v[64:67]\n\t"
      "ds_read_b128 v[172:175], v35 offset:22528\n\t"
      "v_mfma_f32_16x16x32_bf16 v[68:71], a[208:211], v[176:179], v[68:71]\n\t"
      "v_mfma_f32_16x16x32_bf16 v[72:75], a[212:215], v[176:179], v[72:75]\n\t"
      "v_mfma_f32_16x16x32_bf16 v[76:79], a[204:207], v[180:183], v[76:79]\n\t"
      "ds_read_b128 v[176:179], v38 offset:16384\n\t"
      "buffer_load_dwordx4 v42, %[rs], m0 offen offset:1024 sc1 lds\n\t"
      "v_mfma_f32_16x16x32_bf16 v[80:83], a[208:211], v[180:183], v[80:83]\n\t"
      "v_mfma_f32_16x16x32_bf16 v[84:87], a[212:215], v[180:183], v[84:87]\n\t"
      "s_waitcnt lgkmcnt(5)\n\t"
      "v_mfma_f32_16x16x32_bf16 v[88:91], a[204:207], v[184:187], v[88:91]\n\t"
      "ds_read_b128 v[180:183], v38 offset:18432\n\t"
      "v_mfma_f32_16x16x32_bf16 v[92:95], a[208:211], v[184:187], v[92:95]\n\t"
      "s_add_u32 m0, s64, 122880\n\t"
      "v_mfma_f32_16x16x32_bf16 v[96:99], a[212:215], v[184:187], v[96:99]\n\t"
      "buffer_load_dwordx4 v42, %[rs], m0 offen sc1 lds\n\t"
      "v_mfma_f32_16x16x32_bf16 v[100:103], a[204:207], v[188:191], v[100:103]\n\t"
      "ds_read_b128 v[184:187], v38 offset:20480\n\t"
      "v_mfma_f32_16x16x32_bf16 v[104:107], a[208:211], v[188:191], v[104:107]\n\t"
      "v_mfma_f32_16x16x32_bf16 v[108:111], a[212:215], v[188:191], v[108:111]\n\t"
      "s_waitcnt lgkmcnt(5)\n\t"
      "v_mfma_f32_16x16x32_bf16 v[64:67], a[216:219], v[160:163], v[64:67]\n\t"
      "ds_read_b128 v[188:191], v38 offset:22528\n\t"
      "v_mfma_f32_16x16x32_bf16 v[68:71], a[220:223], v[160:163], v[68:71]\n\t"
      "v_cmp_eq_u32 vcc, -1, v49\n\t"
      "buffer_load_dwordx4 v42, %[rs], m0 offen offset:1024 sc1 lds\n\t"
      "v_mfma_f32_16x16x32_bf16 v[72:75], a[224:227], v[160:163], v[72:75]\n\t"
      "s_cbranch_vccnz .Ls1_g5_%=\n\t"
      ".Lr1_g5_%=:\n\t"
      "s_barrier\n\t"
      "v_mfma_f32_16x16x32_bf16 v[76:79], a[216:219], v[164:167], v[76:79]\n\t"
      "ds_read_b128 v[160:163], v33 offset:8192\n\t"
      "v_mfma_f32_16x16x32_bf16 v[80:83], a[220:223], v[164:167], v[80:83]\n\t"
      "v_mfma_f32_16x16x32_bf16 v[84:87], a[224:227], v[164:167], v[84:87]\n\t"
      "s_add_u32 m0, s64, 139264\n\t"
      "s_waitcnt lgkmcnt(5)\n\t"
      "v_mfma_f32_16x16x32_bf16 v[88:91], a[216:219], v[168:171], v[88:91]\n\t"
      "ds_read_b128 v[164:167], v33 offset:10240\n\t"
      "buffer_load_dwordx4 v42, %[rs], m0 offen sc1 lds\n\t"
      "v_mfma_f32_16x16x32_bf16 v[92:95], a[220:223], v[168:171], v[92:95]\n\t"
      "v_mfma_f32_16x16x32_bf16 v[96:99], a[224:227], v[168:171], v[96:99]\n\t"
      "v_mfma_f32_16x16x32_bf16 v[100:103], a[216:219], v[172:175], v[100:103]\n\t"
      "ds_read_b128 v[168:171], v33 offset:12288\n\t"
      "v_mfma_f32_16x16x32_bf16 v[104:107], a[220:223], v[172:175], v[104:107]\n\t"
      "v_mfma_f32_16x16x32_bf16 v[108:111], a[224:227], v[172:175], v[108:111]\n\t"
      "buffer_load_dwordx4 v42, %[rs], m0 offen offset:1024 sc1 lds\n\t"
      "s_waitcnt lgkmcnt(5)\n\t"
      "v_mfma_f32_16x16x32_bf16 v[64:67], a[228:231], v[176:179], v[64:67]\n\t"
      "ds_read_b128 v[172:175], v33 offset:14336\n\t"
      "v_mfma_f32_16x16x32_bf16 v[68:71], a[232:235], v[176:179], v[68:71]\n\t"
      "v_mfma_f32_16x16x32_bf16 v[72:75], a[236:239], v[176:179], v[72:75]\n\t"
      "v_mfma_f32_16x16x32_bf16 v[76:79], a[228:231], v[180:183], v[76:79]\n\t"
      "ds_read_b128 v[176:179], v36 offset:8192\n\t"
      "s_add_u32 m0, s64, 155648\n\t"
      "v_mfma_f32_16x16x32_bf16 v[80:83], a[232:235], v[180:183], v[80:83]\n\t"
      "buffer_load_dwordx4 v42, %[rs], m0 offen sc1 lds\n\t"
      "v_mfma_f32_16x16x32_bf16 v[84:87], a[236:239], v[180:183], v[84:87]\n\t"
      "s_waitcnt lgkmcnt(5)\n\t"
      "v_mfma_f32_16x16x32_bf16 v[88:91], a[228:231], v[184:187], v[88:91]\n\t"
      "ds_read_b128 v[180:183], v36 offset:10240\n\t"
      "v_mfma_f32_16x16x32_bf16 v[92:95], a[232:235], v[184:187], v[92:95]\n\t"
      "v_mfma_f32_16x16x32_bf16 v[96:99], a[236:239], v[184:187], v[96:99]\n\t"
      "v_mfma_f32_16x16x32_bf16 v[100:103], a[228:231], v[188:191], v[100:103]\n\t"
      "ds_read_b128 v[184:187], v36 offset:12288\n\t"
      "buffer_load_dwordx4 v42, %[rs], m0 offen offset:1024 sc1 lds\n\t"
      "v_mfma_f32_16x16x32_bf16 v[104:107], a[232:235], v[188:191], v[104:107]\n\t"
      "v_mfma_f32_16x16x32_bf16 v[108:111], a[236:239], v[188:191], v[108:111]\n\t"
      "s_waitcnt lgkmcnt(5)\n\t"
      "v_mfma_f32_16x16x32_bf16 v[112:115], a[0:3], v[160:163], 0\n\t"
      "ds_read_b128 v[188:191], v36 offset:14336\n\t"
      "v_mfma_f32_16x16x32_bf16 v[116:119], a[4:7], v[160:163], 0\n\t"
      "v_mfma_f32_16x16x32_bf16 v[120:123], a[8:11], v[160:163], 0\n\t"
      "v_mfma_f32_16x16x32_bf16 v[124:127], a[0:3], v[164:167], 0\n\t"
      "ds_read_b128 v[160:163], v33 offset:24576\n\t"
      "v_mfma_f32_16x16x32_bf16 v[128:131], a[4:7], v[164:167], 0\n\t"
      "v_mfma_f32_16x16x32_bf16 v[132:135], a[8:11], v[164:167], 0\n\t"
      "s_waitcnt lgkmcnt(5)\n\t"
      "v_mfma_f32_16x16x32_bf16 v[136:139], a[0:3], v[168:171], 0\n\t"
      "ds_read_b128 v[164:167], v33 offset:26624\n\t"
      "v_mfma_f32_16x16x32_bf16 v[140:143], a[4:7], v[168:171], 0\n\t"
      "v_mfma_f32_16x16x32_bf16 v[144:147], a[8:11], v[168:171], 0\n\t"
      "v_mfma_f32_16x16x32_bf16 v[148:151], a[0:3], v[172:175], 0\n\t"
      "ds_read_b128 v[168:171], v33 offset:28672\n\t"
      "v_max_f32 v64, 0, v64\n\t"
      "v_mfma_f32_16x16x32_bf16 v[152:155], a[4:7], v[172:175], 0\n\t"
      "buffer_load_dwordx4 v[60:63], v52, %[rs], 0 offen sc1\n\t"
      "v_mfma_f32_16x16x32_bf16 v[156:159], a[8:11], v[172:175], 0\n\t"
      "v_max_f32 v65, 0, v65\n\t"
      "s_waitcnt lgkmcnt(5)\n\t"
      "v_mfma_f32_16x16x32_bf16 v[112:115], a[12:15], v[176:179], v[112:115]\n\t"
      "ds_read_b128 v[172:175], v33 offset:30720\n\t"
      "v_mul_f32 v64, v64, v66\n\t"
      "v_mfma_f32_16x16x32_bf16 v[116:119], a[16:19], v[176:179], v[116:119]\n\t"
      "s_add_u32 s61, %[w2b], 16384\n\t"
      "v_mfma_f32_16x16x32_bf16 v[120:123], a[20:23], v[176:179], v[120:123]\n\t"
      "v_mul_f32 v65, v65, v67\n\t"
      "v_cvt_pk_bf16_f32 v192, v64, v65\n\t"
      "v_mfma_f32_16x16x32_bf16 v[124:127], a[12:15], v[180:183], v[124:127]\n\t"
      "ds_read_b128 v[176:179], v36 offset:24576\n\t"
      "v_max_f32 v76, 0, v76\n\t"
      "v_mfma_f32_16x16x32_bf16 v[128:131], a[16:19], v[180:183], v[128:131]\n\t"
      "v_max_f32 v77, 0, v77\n\t"
      "v_mul_f32 v76, v76, v78\n\t"
      "v_mfma_f32_16x16x32_bf16 v[132:135], a[20:23], v[180:183], v[132:135]\n\t"
      "v_mul_f32 v77, v77, v79\n\t"
      "s_waitcnt lgkmcnt(5)\n\t"
      "v_mfma_f32_16x16x32_bf16 v[136:139], a[12:15], v[184:187], v[136:139]\n\t"
      "ds_read_b128 v[180:183], v36 offset:26624\n\t"
      "v_cvt_pk_bf16_f32 v193, v76, v77\n\t"
      "v_mfma_f32_16x16x32_bf16 v[140:143], a[16:19], v[184:187], v[140:143]\n\t"
      "v_max_f32 v88, 0, v88\n\t"
      "v_max_f32 v89, 0, v89\n\t"
      "v_mfma_f32_16x16x32_bf16 v[144:147], a[20:23], v[184:187], v[144:147]\n\t"
      "buffer_load_dwordx4 a[0:3], %[wv], %[rs], s61 offen offset:0 sc1\n\t"
      "v_mfma_f32_16x16x32_bf16 v[148:151], a[12:15], v[188:191], v[148:151]\n\t"
      "ds_read_b128 v[184:187], v36 offset:28672\n\t"
      "v_mul_f32 v88, v88, v90\n\t"
      "v_mfma_f32_16x16x32_bf16 v[152:155], a[16:19], v[188:191], v[152:155]\n\t"
      "v_mul_f32 v89, v89, v91\n\t"
      "v_cvt_pk_bf16_f32 v194, v88, v89\n\t"
      "v_mfma_f32_16x16x32_bf16 v[156:159], a[20:23], v[188:191], v[156:159]\n\t"
      "v_max_f32 v100, 0, v100\n\t"
      "s_waitcnt lgkmcnt(5)\n\t"
      "v_mfma_f32_16x16x32_bf16 v[112:115], a[24:27], v[160:163], v[112:115]\n\t"
      "ds_read_b128 v[188:191], v36 offset:30720\n\t"
      "v_max_f32 v101, 0, v101\n\t"
      "v_mfma_f32_16x16x32_bf16 v[116:119], a[28:31], v[160:163], v[116:119]\n\t"
      "v_mul_f32 v100, v100, v102\n\t"
      "v_mul_f32 v101, v101, v103\n\t"
      "v_mfma_f32_16x16x32_bf16 v[120:123], a[32:35], v[160:163], v[120:123]\n\t"
      "v_cvt_pk_bf16_f32 v195, v100, v101\n\t"
      "v_max_f32 v68, 0, v68\n\t"
      "v_mfma_f32_16x16x32_bf16 v[124:127], a[24:27], v[164:167], v[124:127]\n\t"
      "ds_read_b128 v[160:163], v33 offset:40960\n\t"
      "v_max_f32 v69, 0, v69\n\t"
      "v_mfma_f32_16x16x32_bf16 v[128:131], a[28:31], v[164:167], v[128:131]\n\t"
      "buffer_load_dwordx4 a[4:7], %[wv], %[rs], s61 offen offset:1024 sc1\n\t"
      "v_mfma_f32_16x16x32_bf16 v[132:135], a[32:35], v[164:167], v[132:135]\n\t"
      "v_permlane32_swap_b32 v192, v194\n\t"
      "s_waitcnt lgkmcnt(5)\n\t"
      "v_mfma_f32_16x16x32_bf16 v[136:139], a[24:27], v[168:171], v[136:139]\n\t"
      "ds_read_b128 v[164:167], v33 offset:43008\n\t"
      "v_permlane32_swap_b32 v193, v195\n\t"
      "v_mfma_f32_16x16x32_bf16 v[140:143], a[28:31], v[168:171], v[140:143]\n\t"
      "v_mul_f32 v68, v68, v70\n\t"
      "v_mul_f32 v69, v69, v71\n\t"
      "v_mfma_f32_16x16x32_bf16 v[144:147], a[32:35], v[168:171], v[144:147]\n\t"
      "v_permlane16_swap_b32 v192, v193\n\t"
      "v_mfma_f32_16x16x32_bf16 v[148:151], a[24:27], v[172:175], v[148:151]\n\t"
      "ds_read_b128 v[168:171], v33 offset:45056\n\t"
      "v_permlane16_swap_b32 v194, v195\n\t"
      "v_mfma_f32_16x16x32_bf16 v[152:155], a[28:31], v[172:175], v[152:155]\n\t"
      "buffer_store_dwordx4 v[192:195], %[ho], %[rs], %[hown] offen offset:0 sc1\n\t"
      "v_mfma_f32_16x16x32_bf16 v[156:159], a[32:35], v[172:175], v[156:159]\n\t"
      "v_cvt_pk_bf16_f32 v196, v68, v69\n\t"
      "s_waitcnt lgkmcnt(5)\n\t"
      "v_mfma_f32_16x16x32_bf16 v[112:115], a[36:39], v[176:179], v[112:115]\n\t"
      "ds_read_b128 v[172:175], v33 offset:47104\n\t"
      "v_max_f32 v80, 0, v80\n\t"
      "v_mfma_f32_16x16x32_bf16 v[116:119], a[40:43], v[176:179], v[116:119]\n\t"
      "buffer_load_dwordx4 a[8:11], %[wv], %[rs], s61 offen offset:2048 sc1\n\t"
      "v_mfma_f32_16x16x32_bf16 v[120:123], a[44:47], v[176:179], v[120:123]\n\t"
      "v_max_f32 v81, 0, v81\n\t"
      "v_mul_f32 v80, v80, v82\n\t"
      "v_mfma_f32_16x16x32_bf16 v[124:127], a[36:39], v[180:183], v[124:127]\n\t"
      "ds_read_b128 v[176:179], v36 offset:40960\n\t"
      "v_mul_f32 v81, v81, v83\n\t"
      "v_mfma_f32_16x16x32_bf16 v[128:131], a[40:43], v[180:183], v[128:131]\n\t"
      "v_cvt_pk_bf16_f32 v197, v80, v81\n\t"
      "v_max_f32 v92, 0, v92\n\t"
      "v_mfma_f32_16x16x32_bf16 v[132:135], a[44:47], v[180:183], v[132:135]\n\t"
      "v_max_f32 v93, 0, v93\n\t"
      "s_waitcnt lgkmcnt(5)\n\t"
      "v_mfma_f32_16x16x32_bf16 v[136:139], a[36:39], v[184:187], v[136:139]\n\t"
      "ds_read_b128 v[180:183], v36 offset:43008\n\t"
      "v_mul_f32 v92, v92, v94\n\t"
      "v_mfma_f32_16x16x32_bf16 v[140:143], a[40:43], v[184:187], v[140:143]\n\t"
      "v_mul_f32 v93, v93, v95\n\t"
      "v_cvt_pk_bf16_f32 v198, v92, v93\n\t"
      "v_mfma_f32_16x16x32_bf16 v[144:147], a[44:47], v[184:187], v[144:147]\n\t"
      "buffer_load_dwordx4 a[12:15], %[wv], %[rs], s61 offen offset:3072 sc1\n\t"
      "v_mfma_f32_16x16x32_bf16 v[148:151], a[36:39], v[188:191], v[148:151]\n\t"
      "ds_read_b128 v[184:187], v36 offset:45056\n\t"
      "v_max_f32 v104, 0, v104\n\t"
      "v_mfma_f32_16x16x32_bf16 v[152:155], a[40:43], v[188:191], v[152:155]\n\t"
      "v_max_f32 v105, 0, v105\n\t"
      "v_mul_f32 v104, v104, v106\n\t"
      "v_mfma_f32_16x16x32_bf16 v[156:159], a[44:47], v[188:191], v[156:159]\n\t"
      "v_mul_f32 v105, v105, v107\n\t"
      "s_waitcnt lgkmcnt(5)\n\t"
      "v_mfma_f32_16x16x32_bf16 v[112:115], a[48:51], v[160:163], v[112:115]\n\t"
      "ds_read_b128 v[188:191], v36 offset:47104\n\t"
      "v_cvt_pk_bf16_f32 v199, v104, v105\n\t"
      "v_mfma_f32_16x16x32_bf16 v[116:119], a[52:55], v[160:163], v[116:119]\n\t"
      "v_max_f32 v72, 0, v72\n\t"
      "v_max_f32 v73, 0, v73\n\t"
      "v_mfma_f32_16x16x32_bf16 v[120:123], a[56:59], v[160:163], v[120:123]\n\t"
      "v_permlane32_swap_b32 v196, v198\n\t"
      "v_mfma_f32_16x16x32_bf16 v[124:127], a[48:51], v[164:167], v[124:127]\n\t"
      "ds_read_b128 v[160:163], v33 offset:57344\n\t"
      "v_permlane32_swap_b32 v197, v199\n\t"
      "v_mfma_f32_16x16x32_bf16 v[128:131], a[52:55], v[164:167], v[128:131]\n\t"
      "s_add_u32 s61, %[w2b], 20480\n\t"
      "buffer_load_dwordx4 a[16:19], %[wv], %[rs], s61 offen offset:0 sc1\n\t"
      "v_mfma_f32_16x16x32_bf16 v[132:135], a[56:59], v[164:167], v[132:135]\n\t"
      "v_permlane16_swap_b32 v196, v197\n\t"
      "s_waitcnt lgkmcnt(5)\n\t"
      "v_mfma_f32_16x16x32_bf16 v[136:139], a[48:51], v[168:171], v[136:139]\n\t"
      "ds_read_b128 v[164:167], v33 offset:59392\n\t"
      "v_permlane16_swap_b32 v198, v199\n\t"
      "v_mfma_f32_16x16x32_bf16 v[140:143], a[52:55], v[168:171], v[140:143]\n\t"
      "buffer_store_dwordx4 v[196:199], %[ho], %[rs], %[hown] offen offset:256 sc1\n\t"
      "v_mfma_f32_16x16x32_bf16 v[144:147], a[56:59], v[168:171], v[144:147]\n\t"
      "v_mul_f32 v72, v72, v74\n\t"
      "v_mul_f32 v73, v73, v75\n\t"
      "v_mfma_f32_16x16x32_bf16 v[148:151], a[48:51], v[172:175], v[148:151]\n\t"
      "ds_read_b128 v[168:171], v33 offset:61440\n\t"
      "v_cvt_pk_bf16_f32 v200, v72, v73\n\t"
      "v_mfma_f32_16x16x32_bf16 v[152:155], a[52:55], v[172:175], v[152:155]\n\t"
      "v_max_f32 v84, 0, v84\n\t"
      "v_max_f32 v85, 0, v85\n\t"
      "v_mfma_f32_16x16x32_bf16 v[156:159], a[56:59], v[172:175], v[156:159]\n\t"
      "v_mul_f32 v84, v84, v86\n\t"
      "s_waitcnt lgkmcnt(5)\n\t"
      "v_mfma_f32_16x16x32_bf16 v[112:115], a[60:63], v[176:179], v[112:115]\n\t"
      "ds_read_b128 v[172:175], v33 offset:63488\n\t"
      "v_mul_f32 v85, v85, v87\n\t"
      "v_mfma_f32_16x16x32_bf16 v[116:119], a[64:67], v[176:179], v[116:119]\n\t"
      "buffer_load_dwordx4 a[20:23], %[wv], %[rs], s61 offen offset:1024 sc1\n\t"
      "v_mfma_f32_16x16x32_bf16 v[120:123], a[68:71], v[176:179], v[120:123]\n\t"
      "v_cvt_pk_bf16_f32 v201, v84, v85\n\t"
      "v_max_f32 v96, 0, v96\n\t"
      "v_mfma_f32_16x16x32_bf16 v[124:127], a[60:63], v[180:183], v[124:127]\n\t"
      "ds_read_b128 v[176:179], v36 offset:57344\n\t"
      "v_max_f32 v97, 0, v97\n\t"
      "v_mfma_f32_16x16x32_bf16 v[128:131], a[64:67], v[180:183], v[128:131]\n\t"
      "v_mul_f32 v96, v96, v98\n\t"
      "v_mul_f32 v97, v97, v99\n\t"
      "v_mfma_f32_16x16x32_bf16 v[132:135], a[68:71], v[180:183], v[132:135]\n\t"
      "v_cvt_pk_bf16_f32 v202, v96, v97\n\t"
      "s_waitcnt lgkmcnt(5)\n\t"
      "v_mfma_f32_16x16x32_bf16 v[136:139], a[60:63], v[184:187], v[136:139]\n\t"
      "ds_read_b128 v[180:183], v36 offset:59392\n\t"
      "v_max_f32 v108, 0, v108\n\t"
      "v_mfma_f32_16x16x32_bf16 v[140:143], a[64:67], v[184:187], v[140:143]\n\t"
      "v_max_f32 v109, 0, v109\n\t"
      "v_mul_f32 v108, v108, v110\n\t"
      "v_mfma_f32_16x16x32_bf16 v[144:147], a[68:71], v[184:187], v[144:147]\n\t"
      "buffer_load_dwordx4 a[24:27], %[wv], %[rs], s61 offen offset:2048 sc1\n\t"
      "v_mfma_f32_16x16x32_bf16 v[148:151], a[60:63], v[188:191], v[148:151]\n\t"
      "ds_read_b128 v[184:187], v36 offset:61440\n\t"
      "v_mul_f32 v109, v109, v111\n\t"
      "v_mfma_f32_16x16x32_bf16 v[152:155], a[64:67], v[188:191], v[152:155]\n\t"
      "v_cvt_pk_bf16_f32 v203, v108, v109\n\t"
      "v_mfma_f32_16x16x32_bf16 v[156:159], a[68:71], v[188:191], v[156:159]\n\t"
      "buffer_load_dwordx4 a[28:31], %[wv], %[rs], s61 offen offset:3072 sc1\n\t"
      "s_waitcnt lgkmcnt(5)\n\t"
      "v_mfma_f32_16x16x32_bf16 v[112:115], a[72:75], v[160:163], v[112:115]\n\t"
      "ds_read_b128 v[188:191], v36 offset:63488\n\t"
      "v_permlane32_swap_b32 v200, v202\n\t"
      "v_mfma_f32_16x16x32_bf16 v[116:119], a[76:79], v[160:163], v[116:119]\n\t"
      "v_permlane32_swap_b32 v201, v203\n\t"
      "v_mfma_f32_16x16x32_bf16 v[120:123], a[80:83], v[160:163], v[120:123]\n\t"
      "s_add_u32 s61, %[w2b], 24576\n\t"
      "buffer_load_dwordx4 a[32:35], %[wv], %[rs], s61 offen offset:0 sc1\n\t"
      "v_mfma_f32_16x16x32_bf16 v[124:127], a[72:75], v[164:167], v[124:127]\n\t"
      "ds_read_b128 v[160:163], v34 offset:8192\n\t"
      "v_permlane16_swap_b32 v200, v201\n\t"
      "v_mfma_f32_16x16x32_bf16 v[128:131], a[76:79], v[164:167], v[128:131]\n\t"
      "buffer_load_dwordx4 a[36:39], %[wv], %[rs], s61 offen offset:1024 sc1\n\t"
      "v_mfma_f32_16x16x32_bf16 v[132:135], a[80:83], v[164:167], v[132:135]\n\t"
      "v_permlane16_swap_b32 v202, v203\n\t"
      "s_waitcnt lgkmcnt(5)\n\t"
      "v_mfma_f32_16x16x32_bf16 v[136:139], a[72:75], v[168:171], v[136:139]\n\t"
      "ds_read_b128 v[164:167], v34 offset:10240\n\t"
      "s_waitcnt vmcnt(12)\n\t"
      "v_xor_b32 v60, %[tag], v60\n\t"
      "v_mfma_f32_16x16x32_bf16 v[140:143], a[76:79], v[168:171], v[140:143]\n\t"
      "v_xor_b32 v61, %[tag], v61\n\t"
      "v_xor_b32 v62, %[tag], v62\n\t"
      "v_mfma_f32_16x16x32_bf16 v[144:147], a[80:83], v[168:171], v[144:147]\n\t"
      "v_xor_b32 v63, %[tag], v63\n\t"
      "v_or3_b32 v60, v60, v61, v62\n\t"
      "v_mfma_f32_16x16x32_bf16 v[148:151], a[72:75], v[172:175], v[148:151]\n\t"
      "ds_read_b128 v[168:171], v34 offset:12288\n\t"
      "v_or_b32 v60, v60, v63\n\t"
      "v_mfma_f32_16x16x32_bf16 v[152:155], a[76:79], v[172:175], v[152:155]\n\t"
      "buffer_store_dwordx4 v[200:203], %[ho], %[rs], %[hown] offen offset:512 sc1\n\t"
      "v_mfma_f32_16x16x32_bf16 v[156:159], a[80:83], v[172:175], v[156:159]\n\t"
      "buffer_load_dwordx4 a[40:43], %[wv], %[rs], s61 offen offset:2048 sc1\n\t"
      "s_waitcnt lgkmcnt(5)\n\t"
      "v_mfma_f32_16x16x32_bf16 v[112:115], a[84:87], v[176:179], v[112:115]\n\t"
      "ds_read_b128 v[172:175], v34 offset:14336\n\t"
      "buffer_load_dwordx4 a[44:47], %[wv], %[rs], s61 offen offset:3072 sc1\n\t"
      "v_mfma_f32_16x16x32_bf16 v[116:119], a[88:91], v[176:179], v[116:119]\n\t"
      "v_mfma_f32_16x16x32_bf16 v[120:123], a[92:95], v[176:179], v[120:123]\n\t"
      "v_mfma_f32_16x16x32_bf16 v[124:127], a[84:87], v[180:183], v[124:127]\n\t"
      "ds_read_b128 v[176:179], v37 offset:8192\n\t"
      "s_add_u32 s61, %[w2b], 28672\n\t"
      "v_mfma_f32_16x16x32_bf16 v[128:131], a[88:91], v[180:183], v[128:131]\n\t"
      "v_cmp_ne_u32 vcc, 0, v60\n\t"
      "buffer_load_dwordx4 a[48:51], %[wv], %[rs], s61 offen offset:0 sc1\n\t"
      "v_mfma_f32_16x16x32_bf16 v[132:135], a[92:95], v[180:183], v[132:135]\n\t"
      "s_cbranch_vccnz .Lsfb_%=\n\t"
      ".Lrfb_%=:\n\t"
      "s_waitcnt lgkmcnt(5)\n\t"
      "v_mfma_f32_16x16x32_bf16 v[136:139], a[84:87], v[184:187], v[136:139]\n\t"
      "ds_read_b128 v[180:183], v37 offset:10240\n\t"
      "v_mfma_f32_16x16x32_bf16 v[140:143], a[88:91], v[184:187], v[140:143]\n\t"
      "ds_read_b128 v[64:67], v40 offset:40960\n\t"
      "ds_read_b128 v[68:71], v40 offset:41984\n\t"
      "v_mfma_f32_16x16x32_bf16 v[144:147], a[92:95], v[184:187], v[144:147]\n\t"
      "ds_read_b128 v[72:75], v40 offset:57344\n\t"
      "ds_read_b128 v[76:79], v40 offset:58368\n\t"
      "v_mfma_f32_16x16x32_bf16 v[148:151], a[84:87], v[188:191], v[148:151]\n\t"
      "ds_read_b128 v[184:187], v37 offset:12288\n\t"
      "ds_read_b128 v[80:83], v41 offset:8192\n\t"
      "v_mfma_f32_16x16x32_bf16 v[152:155], a[88:91], v[188:191], v[152:155]\n\t"
      "ds_read_b128 v[84:87], v41 offset:9216\n\t"
      "ds_read_b128 v[88:91], v41 offset:24576\n\t"
      "v_mfma_f32_16x16x32_bf16 v[156:159], a[92:95], v[188:191], v[156:159]\n\t"
      "ds_read_b128 v[92:95], v41 offset:25600\n\t"
      "s_waitcnt lgkmcnt(13)\n\t"
      "v_mfma_f32_16x16x32_bf16 v[112:115], a[96:99], v[160:163], v[112:115]\n\t"
      "ds_read_b128 v[188:191], v37 offset:14336\n\t"
      "buffer_load_dwordx4 a[52:55], %[wv], %[rs], s61 offen offset:1024 sc1\n\t"
      "v_mfma_f32_16x16x32_bf16 v[116:119], a[100:103], v[160:163], v[116:119]\n\t"
      "buffer_load_dwordx4 a[56:59], %[wv], %[rs], s61 offen offset:2048 sc1\n\t"
      "v_mfma_f32_16x16x32_bf16 v[120:123], a[104:107], v[160:163], v[120:123]\n\t"
      "s_waitcnt lgkmcnt(9)\n\t"
      "v_max3_u32 v49, v67, v64, v65\n\t"
      "v_max_u32 v49, v49, v66\n\t"
      "v_mfma_f32_16x16x32_bf16 v[124:127], a[96:99], v[164:167], v[124:127]\n\t"
      "ds_read_b128 v[160:163], v34 offset:24576\n\t"
      "s_waitcnt lgkmcnt(9)\n\t"
      "v_max3_u32 v49, v49, v68, v69\n\t"
      "v_mfma_f32_16x16x32_bf16 v[128:131], a[100:103], v[164:167], v[128:131]\n\t"
      "v_max3_u32 v49, v49, v70, v71\n\t"
      "s_waitcnt lgkmcnt(8)\n\t"
      "v_max3_u32 v49, v49, v72, v73\n\t"
      "v_mfma_f32_16x16x32_bf16 v[132:135], a[104:107], v[164:167], v[132:135]\n\t"
      "v_max3_u32 v49, v49, v74, v75\n\t"
      "v_mfma_f32_16x16x32_bf16 v[136:139], a[96:99], v[168:171], v[136:139]\n\t"
      "ds_read_b128 v[164:167], v34 offset:26624\n\t"
      "s_waitcnt lgkmcnt(8)\n\t"
      "v_max3_u32 v49, v49, v76, v77\n\t"
      "v_mfma_f32_16x16x32_bf16 v[140:143], a[100:103], v[168:171], v[140:143]\n\t"
      "v_max3_u32 v49, v49, v78, v79\n\t"
      "s_waitcnt lgkmcnt(6)\n\t"
      "v_max3_u32 v49, v49, v80, v81\n\t"
      "v_mfma_f32_16x16x32_bf16 v[144:147], a[104:107], v[168:171], v[144:147]\n\t"
      "v_max3_u32 v49, v49, v82, v83\n\t"
      "s_waitcnt lgkmcnt(5)\n\t"
      "v_max3_u32 v49, v49, v84, v85\n\t"
      "v_mfma_f32_16x16x32_bf16 v[148:151], a[96:99], v[172:175], v[148:151]\n\t"
      "ds_read_b128 v[168:171], v34 offset:28672\n\t"
      "v_max3_u32 v49, v49, v86, v87\n\t"
      "v_mfma_f32_16x16x32_bf16 v[152:155], a[100:103], v[172:175], v[152:155]\n\t"
      "s_waitcnt lgkmcnt(5)\n\t"
      "v_max3_u32 v49, v49, v88, v89\n\t"
      "v_max3_u32 v49, v49, v90, v91\n\t"
      "v_mfma_f32_16x16x32_bf16 v[156:159], a[104:107], v[172:175], v[156:159]\n\t"
      "s_waitcnt lgkmcnt(4)\n\t"
      "v_max3_u32 v49, v49, v92, v93\n\t"
      "v_mfma_f32_16x16x32_bf16 v[112:115], a[108:111], v[176:179], v[112:115]\n\t"
      "ds_read_b128 v[172:175], v34 offset:30720\n\t"
      "v_max3_u32 v49, v49, v94, v95\n\t"
      "v_mfma_f32_16x16x32_bf16 v[116:119], a[112:115], v[176:179], v[116:119]\n\t"
      "buffer_load_dwordx4 a[60:63], %[wv], %[rs], s61 offen offset:3072 sc1\n\t"
      "v_mfma_f32_16x16x32_bf16 v[120:123], a[116:119], v[176:179], v[120:123]\n\t"
      "s_add_u32 s61, %[w2b], 32768\n\t"
      "buffer_load_dwordx4 a[64:67], %[wv], %[rs], s61 offen offset:0 sc1\n\t"
      "v_mfma_f32_16x16x32_bf16 v[124:127], a[108:111], v[180:183], v[124:127]\n\t"
      "ds_read_b128 v[176:179], v37 offset:24576\n\t"
      "v_mfma_f32_16x16x32_bf16 v[128:131], a[112:115], v[180:183], v[128:131]\n\t"
      "v_mfma_f32_16x16x32_bf16 v[132:135], a[116:119], v[180:183], v[132:135]\n\t"
      "buffer_load_dwordx4 a[68:71], %[wv], %[rs], s61 offen offset:1024 sc1\n\t"
      "s_waitcnt lgkmcnt(5)\n\t"
      "v_mfma_f32_16x16x32_bf16 v[136:139], a[108:111], v[184:187], v[136:139]\n\t"
      "ds_read_b128 v[180:183], v37 offset:26624\n\t"
      "v_mfma_f32_16x16x32_bf16 v[140:143], a[112:115], v[184:187], v[140:143]\n\t"
      "v_mfma_f32_16x16x32_bf16 v[144:147], a[116:119], v[184:187], v[144:147]\n\t"
      "v_mfma_f32_16x16x32_bf16 v[148:151], a[108:111], v[188:191], v[148:151]\n\t"
      "ds_read_b128 v[184:187], v37 offset:28672\n\t"
      "buffer_load_dwordx4 a[72:75], %[wv], %[rs], s61 offen offset:2048 sc1\n\t"
      "v_mfma_f32_16x16x32_bf16 v[152:155], a[112:115], v[188:191], v[152:155]\n\t"
      "v_mfma_f32_16x16x32_bf16 v[156:159], a[116:119], v[188:191], v[156:159]\n\t"
      "s_waitcnt lgkmcnt(5)\n\t"
      "v_mfma_f32_16x16x32_bf16 v[112:115], a[120:123], v[160:163], v[112:115]\n\t"
      "ds_read_b128 v[188:191], v37 offset:30720\n\t"
      "v_mfma_f32_16x16x32_bf16 v[116:119], a[124:127], v[160:163], v[116:119]\n\t"
      "v_cmp_eq_u32 vcc, -1, v49\n\t"
      "buffer_load_dwordx4 a[76:79], %[wv], %[rs], s61 offen offset:3072 sc1\n\t"
      "v_mfma_f32_16x16x32_bf16 v[120:123], a[128:131], v[160:163], v[120:123]\n\t"
      "s_cbranch_vccnz .Ls1_g8_%=\n\t"
      ".Lr1_g8_%=:\n\t"
      "s_barrier\n\t"
      "v_mfma_f32_16x16x32_bf16 v[124:127], a[120:123], v[164:167], v[124:127]\n\t"
      "ds_read_b128 v[160:163], v34 offset:40960\n\t"
      "v_mfma_f32_16x16x32_bf16 v[128:131], a[124:127], v[164:167], v[128:131]\n\t"
      "s_add_u32 s61, %[w2b], 36864\n\t"
      "v_mfma_f32_16x16x32_bf16 v[132:135], a[128:131], v[164:167], v[132:135]\n\t"
      "buffer_load_dwordx4 a[80:83], %[wv], %[rs], s61 offen offset:0 sc1\n\t"
      "s_waitcnt lgkmcnt(5)\n\t"
      "v_mfma_f32_16x16x32_bf16 v[136:139], a[120:123], v[168:171], v[136:139]\n\t"
      "ds_read_b128 v[164:167], v34 offset:43008\n\t"
      "v_mfma_f32_16x16x32_bf16 v[140:143], a[124:127], v[168:171], v[140:143]\n\t"
      "v_mfma_f32_16x16x32_bf16 v[144:147], a[128:131], v[168:171], v[144:147]\n\t"
      "s_add_u32 s62, %[hb], 0\n\t"
      "v_mfma_f32_16x16x32_bf16 v[148:151], a[120:123], v[172:175], v[148:151]\n\t"
      "ds_read_b128 v[168:171], v34 offset:45056\n\t"
      "buffer_load_dwordx4 v[64:67], v43, %[rs], s62 offen offset:0 sc1\n\t"
      "v_mfma_f32_16x16x32_bf16 v[152:155], a[124:127], v[172:175], v[152:155]\n\t"
      "v_mfma_f32_16x16x32_bf16 v[156:159], a[128:131], v[172:175], v[156:159]\n\t"
      "s_waitcnt lgkmcnt(5)\n\t"
      "v_mfma_f32_16x16x32_bf16 v[112:115], a[132:135], v[176:179], v[112:115]\n\t"
      "ds_read_b128 v[172:175], v34 offset:47104\n\t"
      "v_mfma_f32_16x16x32_bf16 v[116:119], a[136:139], v[176:179], v[116:119]\n\t"
      "buffer_load_dwordx4 v[68:71], v44, %[rs], s62 offen offset:0 sc1\n\t"
      "v_mfma_f32_16x16x32_bf16 v[120:123], a[140:143], v[176:179], v[120:123]\n\t"
      "v_mfma_f32_16x16x32_bf16 v[124:127], a[132:135], v[180:183], v[124:127]\n\t"
      "ds_read_b128 v[176:179], v37 offset:40960\n\t"
      "v_mfma_f32_16x16x32_bf16 v[128:131], a[136:139], v[180:183], v[128:131]\n\t"
      "v_mfma_f32_16x16x32_bf16 v[132:135], a[140:143], v[180:183], v[132:135]\n\t"
      "buffer_load_dwordx4 v[72:75], v43, %[rs], s62 offen offset:1024 sc1\n\t"
      "s_waitcnt lgkmcnt(5)\n\t"
      "v_mfma_f32_16x16x32_bf16 v[136:139], a[132:135], v[184:187], v[136:139]\n\t"
      "ds_read_b128 v[180:183], v37 offset:43008\n\t"
      "v_mfma_f32_16x16x32_bf16 v[140:143], a[136:139], v[184:187], v[140:143]\n\t"
      "v_mfma_f32_16x16x32_bf16 v[144:147], a[140:143], v[184:187], v[144:147]\n\t"
      "buffer_load_dwordx4 v[76:79], v44, %[rs], s62 offen offset:1024 sc1\n\t"
      "v_mfma_f32_16x16x32_bf16 v[148:151], a[132:135], v[188:191], v[148:151]\n\t"
      "ds_read_b128 v[184:187], v37 offset:45056\n\t"
      "v_mfma_f32_16x16x32_bf16 v[152:155], a[136:139], v[188:191], v[152:155]\n\t"
      "v_mfma_f32_16x16x32_bf16 v[156:159], a[140:143], v[188:191], v[156:159]\n\t"
      "s_waitcnt lgkmcnt(5)\n\t"
      "v_mfma_f32_16x16x32_bf16 v[112:115], a[144:147], v[160:163], v[112:115]\n\t"
      "ds_read_b128 v[188:191], v37 offset:47104\n\t"
      "buffer_load_dwordx4 v[80:83], v43, %[rs], s62 offen offset:2048 sc1\n\t"
      "v_mfma_f32_16x16x32_bf16 v[116:119], a[148:151], v[160:163], v[116:119]\n\t"
      "v_mfma_f32_16x16x32_bf16 v[120:123], a[152:155], v[160:163], v[120:123]\n\t"
      "v_mfma_f32_16x16x32_bf16 v[124:127], a[144:147], v[164:167], v[124:127]\n\t"
      "ds_read_b128 v[160:163], v34 offset:57344\n\t"
      "v_mfma_f32_16x16x32_bf16 v[128:131], a[148:151], v[164:167], v[128:131]\n\t"
      "buffer_load_dwordx4 v[84:87], v44, %[rs], s62 offen offset:2048 sc1\n\t"
      "v_mfma_f32_16x16x32_bf16 v[132:135], a[152:155], v[164:167], v[132:135]\n\t"
      "s_waitcnt lgkmcnt(5)\n\t"
      "v_mfma_f32_16x16x32_bf16 v[136:139], a[144:147], v[168:171], v[136:139]\n\t"
      "ds_read_b128 v[164:167], v34 offset:59392\n\t"
      "v_mfma_f32_16x16x32_bf16 v[140:143], a[148:151], v[168:171], v[140:143]\n\t"
      "s_add_u32 s62, %[hb], 24576\n\t"
      "v_mfma_f32_16x16x32_bf16 v[144:147], a[152:155], v[168:171], v[144:147]\n\t"
      "buffer_load_dwordx4 v[88:91], v43, %[rs], s62 offen offset:0 sc1\n\t"
      "v_mfma_f32_16x16x32_bf16 v[148:151], a[144:147], v[172:175], v[148:151]\n\t"
      "ds_read_b128 v[168:171], v34 offset:61440\n\t"
      "v_mfma_f32_16x16x32_bf16 v[152:155], a[148:151], v[172:175], v[152:155]\n\t"
      "v_mfma_f32_16x16x32_bf16 v[156:159], a[152:155], v[172:175], v[156:159]\n\t"
      "s_waitcnt lgkmcnt(5)\n\t"
      "v_mfma_f32_16x16x32_bf16 v[112:115], a[156:159], v[176:179], v[112:115]\n\t"
      "ds_read_b128 v[172:175], v34 offset:63488\n\t"
      "buffer_load_dwordx4 v[92:95], v44, %[rs], s62 offen offset:0 sc1\n\t"
      "v_mfma_f32_16x16x32_bf16 v[116:119], a[160:163], v[176:179], v[116:119]\n\t"
      "v_mfma_f32_16x16x32_bf16 v[120:123], a[164:167], v[176:179], v[120:123]\n\t"
      "v_mfma_f32_16x16x32_bf16 v[124:127], a[156:159], v[180:183], v[124:127]\n\t"
      "ds_read_b128 v[176:179], v37 offset:57344\n\t"
      "v_mfma_f32_16x16x32_bf16 v[128:131], a[160:163], v[180:183], v[128:131]\n\t"
      "buffer_load_dwordx4 v[96:99], v43, %[rs], s62 offen offset:1024 sc1\n\t"
      "v_mfma_f32_16x16x32_bf16 v[132:135], a[164:167], v[180:183], v[132:135]\n\t"
      "s_waitcnt lgkmcnt(5)\n\t"
      "v_mfma_f32_16x16x32_bf16 v[136:139], a[156:159], v[184:187], v[136:139]\n\t"
      "ds_read_b128 v[180:183], v37 offset:59392\n\t"
      "v_mfma_f32_16x16x32_bf16 v[140:143], a[160:163], v[184:187], v[140:143]\n\t"
      "v_mfma_f32_16x16x32_bf16 v[144:147], a[164:167], v[184:187], v[144:147]\n\t"
      "buffer_load_dwordx4 v[100:103], v44, %[rs], s62 offen offset:1024 sc1\n\t"
      "v_mfma_f32_16x16x32_bf16 v[148:151], a[156:159], v[188:191], v[148:151]\n\t"
      "ds_read_b128 v[184:187], v37 offset:61440\n\t"
      "v_mfma_f32_16x16x32_bf16 v[152:155], a[160:163], v[188:191], v[152:155]\n\t"
      "v_mfma_f32_16x16x32_bf16 v[156:159], a[164:167], v[188:191], v[156:159]\n\t"
      "s_waitcnt lgkmcnt(5)\n\t"
      "v_mfma_f32_16x16x32_bf16 v[112:115], a[168:171], v[160:163], v[112:115]\n\t"
      "ds_read_b128 v[188:191], v37 offset:63488\n\t"
      "buffer_load_dwordx4 v[104:107], v43, %[rs], s62 offen offset:2048 sc1\n\t"
      "v_mfma_f32_16x16x32_bf16 v[116:119], a[172:175], v[160:163], v[116:119]\n\t"
      "v_mfma_f32_16x16x32_bf16 v[120:123], a[176:179], v[160:163], v[120:123]\n\t"
      "v_mfma_f32_16x16x32_bf16 v[124:127], a[168:171], v[164:167], v[124:127]\n\t"
      "ds_read_b128 v[160:163], v35 offset:8192\n\t"
      "buffer_load_dwordx4 v[108:111], v44, %[rs], s62 offen offset:2048 sc1\n\t"
      "v_mfma_f32_16x16x32_bf16 v[128:131], a[172:175], v[164:167], v[128:131]\n\t"
      "v_mfma_f32_16x16x32_bf16 v[132:135], a[176:179], v[164:167], v[132:135]\n\t"
      "s_waitcnt lgkmcnt(5)\n\t"
      "v_mfma_f32_16x16x32_bf16 v[136:139], a[168:171], v[168:171], v[136:139]\n\t"
      "ds_read_b128 v[164:167], v35 offset:10240\n\t"
      "v_mfma_f32_16x16x32_bf16 v[140:143], a[172:175], v[168:171], v[140:143]\n\t"
      "buffer_load_dwordx4 a[84:87], %[wv], %[rs], s61 offen offset:1024 sc1\n\t"
      "v_mfma_f32_16x16x32_bf16 v[144:147], a[176:179], v[168:171], v[144:147]\n\t"
      "v_mfma_f32_16x16x32_bf16 v[148:151], a[168:171], v[172:175], v[148:151]\n\t"
      "ds_read_b128 v[168:171], v35 offset:12288\n\t"
      "v_mfma_f32_16x16x32_bf16 v[152:155], a[172:175], v[172:175], v[152:155]\n\t"
      "v_mfma_f32_16x16x32_bf16 v[156:159], a[176:179], v[172:175], v[156:159]\n\t"
      "buffer_load_dwordx4 a[88:91], %[wv], %[rs], s61 offen offset:2048 sc1\n\t"
      "s_waitcnt lgkmcnt(5)\n\t"
      "v_mfma_f32_16x16x32_bf16 v[112:115], a[180:183], v[176:179], v[112:115]\n\t"
      "ds_read_b128 v[172:175], v35 offset:14336\n\t"
      "v_mfma_f32_16x16x32_bf16 v[116:119], a[184:187], v[176:179], v[116:119]\n\t"
      "v_mfma_f32_16x16x32_bf16 v[120:123], a[188:191], v[176:179], v[120:123]\n\t"
      "v_accvgpr_write_b32 a176, v208\n\t"
      "v_accvgpr_write_b32 a177, v209\n\t"
      "v_mfma_f32_16x16x32_bf16 v[124:127], a[180:183], v[180:183], v[124:127]\n\t"
      "ds_read_b128 v[176:179], v38 offset:8192\n\t"
      "v_accvgpr_write_b32 a178, v210\n\t"
      "v_mfma_f32_16x16x32_bf16 v[128:131], a[184:187], v[180:183], v[128:131]\n\t"
      "v_accvgpr_write_b32 a179, v211\n\t"
      "buffer_load_dwordx4 a[92:95], %[wv], %[rs], s61 offen offset:3072 sc1\n\t"
      "v_mfma_f32_16x16x32_bf16 v[132:135], a[188:191], v[180:183], v[132:135]\n\t"
      "s_waitcnt lgkmcnt(5)\n\t"
      "v_mfma_f32_16x16x32_bf16 v[136:139], a[180:183], v[184:187], v[136:139]\n\t"
      "ds_read_b128 v[180:183], v38 offset:10240\n\t"
      "s_add_u32 s61, %[w2b], 40960\n\t"
      "v_mfma_f32_16x16x32_bf16 v[140:143], a[184:187], v[184:187], v[140:143]\n\t"
      "buffer_load_dwordx4 a[96:99], %[wv], %[rs], s61 offen offset:0 sc1\n\t"
      "v_mfma_f32_16x16x32_bf16 v[144:147], a[188:191], v[184:187], v[144:147]\n\t"
      "v_mfma_f32_16x16x32_bf16 v[148:151], a[180:183], v[188:191], v[148:151]\n\t"
      "ds_read_b128 v[184:187], v38 offset:12288\n\t"
      "v_mfma_f32_16x16x32_bf16 v[152:155], a[184:187], v[188:191], v[152:155]\n\t"
      "v_mfma_f32_16x16x32_bf16 v[156:159], a[188:191], v[188:191], v[156:159]\n\t"
      "buffer_load_dwordx4 a[100:103], %[wv], %[rs], s61 offen offset:1024 sc1\n\t"
      "s_waitcnt lgkmcnt(5)\n\t"
      "v_mfma_f32_16x16x32_bf16 v[112:115], a[192:195], v[160:163], v[112:115]\n\t"
      "ds_read_b128 v[188:191], v38 offset:14336\n\t"
      "v_mfma_f32_16x16x32_bf16 v[116:119], a[196:199], v[160:163], v[116:119]\n\t"
      "v_mfma_f32_16x16x32_bf16 v[120:123], a[200:203], v[160:163], v[120:123]\n\t"
      "v_accvgpr_write_b32 a180, v212\n\t"
      "v_accvgpr_write_b32 a181, v213\n\t"
      "v_mfma_f32_16x16x32_bf16 v[124:127], a[192:195], v[164:167], v[124:127]\n\t"
      "ds_read_b128 v[160:163], v35 offset:24576\n\t"
      "v_accvgpr_write_b32 a182, v214\n\t"
      "v_mfma_f32_16x16x32_bf16 v[128:131], a[196:199], v[164:167], v[128:131]\n\t"
      "v_accvgpr_write_b32 a183, v215\n\t"
      "v_accvgpr_write_b32 a184, v216\n\t"
      "v_mfma_f32_16x16x32_bf16 v[132:135], a[200:203], v[164:167], v[132:135]\n\t"
      "v_accvgpr_write_b32 a185, v217\n\t"
      "s_waitcnt lgkmcnt(5)\n\t"
      "v_mfma_f32_16x16x32_bf16 v[136:139], a[192:195], v[168:171], v[136:139]\n\t"
      "ds_read_b128 v[164:167], v35 offset:26624\n\t"
      "v_accvgpr_write_b32 a186, v218\n\t"
      "v_mfma_f32_16x16x32_bf16 v[140:143], a[196:199], v[168:171], v[140:143]\n\t"
      "v_accvgpr_write_b32 a187, v219\n\t"
      "v_accvgpr_write_b32 a188, v220\n\t"
      "v_mfma_f32_16x16x32_bf16 v[144:147], a[200:203], v[168:171], v[144:147]\n\t"
      "v_accvgpr_write_b32 a189, v221\n\t"
      "v_accvgpr_write_b32 a190, v222\n\t"
      "v_mfma_f32_16x16x32_bf16 v[148:151], a[192:195], v[172:175], v[148:151]\n\t"
      "ds_read_b128 v[168:171], v35 offset:28672\n\t"
      "v_accvgpr_write_b32 a191, v223\n\t"
      "v_mfma_f32_16x16x32_bf16 v[152:155], a[196:199], v[172:175], v[152:155]\n\t"
      "buffer_load_dwordx4 a[104:107], %[wv], %[rs], s61 offen offset:2048 sc1\n\t"
      "v_mfma_f32_16x16x32_bf16 v[156:159], a[200:203], v[172:175], v[156:159]\n\t"
      "buffer_load_dwordx4 a[108:111], %[wv], %[rs], s61 offen offset:3072 sc1\n\t"
      "s_waitcnt lgkmcnt(5)\n\t"
      "v_mfma_f32_16x16x32_bf16 v[112:115], a[204:207], v[176:179], v[112:115]\n\t"
      "ds_read_b128 v[172:175], v35 offset:30720\n\t"
      "v_mfma_f32_16x16x32_bf16 v[116:119], a[208:211], v[176:179], v[116:119]\n\t"
      "s_add_u32 s61, %[w2b], 45056\n\t"
      "v_mfma_f32_16x16x32_bf16 v[120:123], a[212:215], v[176:179], v[120:123]\n\t"
      "v_accvgpr_write_b32 a192, v224\n\t"
      "v_accvgpr_write_b32 a193, v225\n\t"
      "v_mfma_f32_16x16x32_bf16 v[124:127], a[204:207], v[180:183], v[124:127]\n\t"
      "ds_read_b128 v[176:179], v38 offset:24576\n\t"
      "v_accvgpr_write_b32 a194, v226\n\t"
      "v_mfma_f32_16x16x32_bf16 v[128:131], a[208:211], v[180:183], v[128:131]\n\t"
      "v_accvgpr_write_b32 a195, v227\n\t"
      "v_accvgpr_write_b32 a196, v228\n\t"
      "v_mfma_f32_16x16x32_bf16 v[132:135], a[212:215], v[180:183], v[132:135]\n\t"
      "v_accvgpr_write_b32 a197, v229\n\t"
      "s_waitcnt lgkmcnt(5)\n\t"
      "v_mfma_f32_16x16x32_bf16 v[136:139], a[204:207], v[184:187], v[136:139]\n\t"
      "ds_read_b128 v[180:183], v38 offset:26624\n\t"
      "v_accvgpr_write_b32 a198, v230\n\t"
      "v_mfma_f32_16x16x32_bf16 v[140:143], a[208:211], v[184:187], v[140:143]\n\t"
      "v_accvgpr_write_b32 a199, v231\n\t"
      "v_accvgpr_write_b32 a200, v232\n\t"
      "v_mfma_f32_16x16x32_bf16 v[144:147], a[212:215], v[184:187], v[144:147]\n\t"
      "v_accvgpr_write_b32 a201, v233\n\t"
      "v_accvgpr_write_b32 a202, v234\n\t"
      "v_mfma_f32_16x16x32_bf16 v[148:151], a[204:207], v[188:191], v[148:151]\n\t"
      "ds_read_b128 v[184:187], v38 offset:28672\n\t"
      "v_accvgpr_write_b32 a203, v235\n\t"
      "v_mfma_f32_16x16x32_bf16 v[152:155], a[208:211], v[188:191], v[152:155]\n\t"
      "buffer_load_dwordx4 a[112:115], %[wv], %[rs], s61 offen offset:0 sc1\n\t"
      "v_mfma_f32_16x16x32_bf16 v[156:159], a[212:215], v[188:191], v[156:159]\n\t"
      "s_add_u32 s62, %[hb], 49152\n\t"
      "s_waitcnt lgkmcnt(5)\n\t"
      "v_mfma_f32_16x16x32_bf16 v[112:115], a[216:219], v[160:163], v[112:115]\n\t"
      "ds_read_b128 v[188:191], v38 offset:30720\n\t"
      "buffer_load_dwordx4 v[208:211], v43, %[rs], s62 offen offset:0 sc1\n\t"
      "v_mfma_f32_16x16x32_bf16 v[116:119], a[220:223], v[160:163], v[116:119]\n\t"
      "v_mfma_f32_16x16x32_bf16 v[120:123], a[224:227], v[160:163], v[120:123]\n\t"
      "v_accvgpr_write_b32 a204, v236\n\t"
      "v_accvgpr_write_b32 a205, v237\n\t"
      "v_mfma_f32_16x16x32_bf16 v[124:127], a[216:219], v[164:167], v[124:127]\n\t"
      "v_accvgpr_write_b32 a206, v238\n\t"
      "v_accvgpr_write_b32 a207, v239\n\t"
      "v_mfma_f32_16x16x32_bf16 v[128:131], a[220:223], v[164:167], v[128:131]\n\t"
      "v_accvgpr_write_b32 a208, v240\n\t"
      "v_accvgpr_write_b32 a209, v241\n\t"
      "v_mfma_f32_16x16x32_bf16 v[132:135], a[224:227], v[164:167], v[132:135]\n\t"
      "v_accvgpr_write_b32 a210, v242\n\t"
      "s_waitcnt lgkmcnt(4)\n\t"
      "v_mfma_f32_16x16x32_bf16 v[136:139], a[216:219], v[168:171], v[136:139]\n\t"
      "v_accvgpr_write_b32 a211, v243\n\t"
      "v_accvgpr_write_b32 a212, v244\n\t"
      "v_mfma_f32_16x16x32_bf16 v[140:143], a[220:223], v[168:171], v[140:143]\n\t"
      "v_accvgpr_write_b32 a213, v245\n\t"
      "v_accvgpr_write_b32 a214, v246\n\t"
      "v_mfma_f32_16x16x32_bf16 v[144:147], a[224:227], v[168:171], v[144:147]\n\t"
      "v_accvgpr_write_b32 a215, v247\n\t"
      "buffer_load_dwordx4 v[212:215], v44, %[rs], s62 offen offset:0 sc1\n\t"
      "v_mfma_f32_16x16x32_bf16 v[148:151], a[216:219], v[172:175], v[148:151]\n\t"
      "buffer_load_dwordx4 v[216:219], v43, %[rs], s62 offen offset:1024 sc1\n\t"
      "v_mfma_f32_16x16x32_bf16 v[152:155], a[220:223], v[172:175], v[152:155]\n\t"
      "v_mfma_f32_16x16x32_bf16 v[156:159], a[224:227], v[172:175], v[156:159]\n\t"
      "s_waitcnt lgkmcnt(2)\n\t"
      "v_mfma_f32_16x16x32_bf16 v[112:115], a[228:231], v[176:179], v[112:115]\n\t"
      "buffer_load_dwordx4 v[220:223], v44, %[rs], s62 offen offset:1024 sc1\n\t"
      "v_mfma_f32_16x16x32_bf16 v[116:119], a[232:235], v[176:179], v[116:119]\n\t"
      "v_mfma_f32_16x16x32_bf16 v[120:123], a[236:239], v[176:179], v[120:123]\n\t"
      "v_accvgpr_write_b32 a216, v248\n\t"
      "v_accvgpr_write_b32 a217, v249\n\t"
      "v_mfma_f32_16x16x32_bf16 v[124:127], a[228:231], v[180:183], v[124:127]\n\t"
      "v_accvgpr_write_b32 a218, v250\n\t"
      "v_accvgpr_write_b32 a219, v251\n\t"
      "v_mfma_f32_16x16x32_bf16 v[128:131], a[232:235], v[180:183], v[128:131]\n\t"
      "v_accvgpr_write_b32 a220, v252\n\t"
      "v_accvgpr_write_b32 a221, v253\n\t"
      "v_mfma_f32_16x16x32_bf16 v[132:135], a[236:239], v[180:183], v[132:135]\n\t"
      "v_accvgpr_write_b32 a222, v254\n\t"
      "s_waitcnt lgkmcnt(0)\n\t"
      "v_mfma_f32_16x16x32_bf16 v[136:139], a[228:231], v[184:187], v[136:139]\n\t"
      "v_accvgpr_write_b32 a223, v255\n\t"
      "buffer_load_dwordx4 v[224:227], v43, %[rs], s62 offen offset:2048 sc1\n\t"
      "v_mfma_f32_16x16x32_bf16 v[140:143], a[232:235], v[184:187], v[140:143]\n\t"
      "buffer_load_dwordx4 v[228:231], v44, %[rs], s62 offen offset:2048 sc1\n\t"
      "v_mfma_f32_16x16x32_bf16 v[144:147], a[236:239], v[184:187], v[144:147]\n\t"
      "v_mfma_f32_16x16x32_bf16 v[148:151], a[228:231], v[188:191], v[148:151]\n\t"
      "v_mfma_f32_16x16x32_bf16 v[152:155], a[232:235], v[188:191], v[152:155]\n\t"
      "s_add_u32 s62, %[hb], 73728\n\t"
      "v_mfma_f32_16x16x32_bf16 v[156:159], a[236:239], v[188:191], v[156:159]\n\t"
      "buffer_load_dwordx4 v[232:235], v43, %[rs], s62 offen offset:0 sc1\n\t"
      "s_nop 6\n\t"
      "s_add_u32 s63, %[hown], 12288\n\t"
      "v_max_f32 v112, 0, v112\n\t"
      "v_max_f32 v113, 0, v113\n\t"
      "v_pk_mul_f32 v[112:113], v[112:113], v[114:115]\n\t"
      "v_cvt_pk_bf16_f32 v192, v112, v113\n\t"
      "v_max_f32 v124, 0, v124\n\t"
      "v_max_f32 v125, 0, v125\n\t"
      "v_pk_mul_f32 v[124:125], v[124:125], v[126:127]\n\t"
      "v_cvt_pk_bf16_f32 v193, v124, v125\n\t"
      "v_max_f32 v136, 0, v136\n\t"
      "v_max_f32 v137, 0, v137\n\t"
      "v_pk_mul_f32 v[136:137], v[136:137], v[138:139]\n\t"
      "v_cvt_pk_bf16_f32 v194, v136, v137\n\t"
      "v_max_f32 v148, 0, v148\n\t"
      "v_max_f32 v149, 0, v149\n\t"
      "v_pk_mul_f32 v[148:149], v[148:149], v[150:151]\n\t"
      "v_cvt_pk_bf16_f32 v195, v148, v149\n\t"
      "v_max_f32 v116, 0, v116\n\t"
      "v_max_f32 v117, 0, v117\n\t"
      "v_permlane32_swap_b32 v192, v194\n\t"
      "v_permlane32_swap_b32 v193, v195\n\t"
      "v_pk_mul_f32 v[116:117], v[116:117], v[118:119]\n\t"
      "v_cvt_pk_bf16_f32 v196, v116, v117\n\t"
      "v_permlane16_swap_b32 v192, v193\n\t"
      "v_permlane16_swap_b32 v194, v195\n\t"
      "buffer_store_dwordx4 v[192:195], %[ho], %[rs], s63 offen offset:0 sc1\n\t"
      "v_max_f32 v128, 0, v128\n\t"
      "v_max_f32 v129, 0, v129\n\t"
      "v_pk_mul_f32 v[128:129], v[128:129], v[130:131]\n\t"
      "v_cvt_pk_bf16_f32 v197, v128, v129\n\t"
      "v_max_f32 v140, 0, v140\n\t"
      "v_max_f32 v141, 0, v141\n\t"
      "v_pk_mul_f32 v[140:141], v[140:141], v[142:143]\n\t"
      "v_cvt_pk_bf16_f32 v198, v140, v141\n\t"
      "v_max_f32 v152, 0, v152\n\t"
      "v_max_f32 v153, 0, v153\n\t"
      "v_pk_mul_f32 v[152:153], v[152:153], v[154:155]\n\t"
      "v_cvt_pk_bf16_f32 v199, v152, v153\n\t"
      "v_max_f32 v120, 0, v120\n\t"
      "v_max_f32 v121, 0, v121\n\t"
      "v_permlane32_swap_b32 v196, v198\n\t"
      "v_permlane32_swap_b32 v197, v199\n\t"
      "v_pk_mul_f32 v[120:121], v[120:121], v[122:123]\n\t"
      "v_cvt_pk_bf16_f32 v200, v120, v121\n\t"
      "v_permlane16_swap_b32 v196, v197\n\t"
      "v_permlane16_swap_b32 v198, v199\n\t"
      "buffer_store_dwordx4 v[196:199], %[ho], %[rs], s63 offen offset:256 sc1\n\t"
      "v_max_f32 v132, 0, v132\n\t"
      "v_max_f32 v133, 0, v133\n\t"
      "v_pk_mul_f32 v[132:133], v[132:133], v[134:135]\n\t"
      "v_cvt_pk_bf16_f32 v201, v132, v133\n\t"
      "v_max_f32 v144, 0, v144\n\t"
      "v_max_f32 v145, 0, v145\n\t"
      "v_pk_mul_f32 v[144:145], v[144:145], v[146:147]\n\t"
      "v_cvt_pk_bf16_f32 v202, v144, v145\n\t"
      "v_max_f32 v156, 0, v156\n\t"
      "v_max_f32 v157, 0, v157\n\t"
      "v_pk_mul_f32 v[156:157], v[156:157], v[158:159]\n\t"
      "v_cvt_pk_bf16_f32 v203, v156, v157\n\t"
      "s_nop 1\n\t"
      "s_nop 0\n\t"
      "v_permlane32_swap_b32 v200, v202\n\t"
      "v_permlane32_swap_b32 v201, v203\n\t"
      "s_nop 1\n\t"
      "s_nop 0\n\t"
      "v_permlane16_swap_b32 v200, v201\n\t"
      "v_permlane16_swap_b32 v202, v203\n\t"
      "buffer_store_dwordx4 v[200:203], %[ho], %[rs], s63 offen offset:512 sc1\n\t"
      "buffer_load_dwordx4 v[236:239], v44, %[rs], s62 offen offset:0 sc1\n\t"
      "buffer_load_dwordx4 v[240:243], v43, %[rs], s62 offen offset:1024 sc1\n\t"
      "buffer_load_dwordx4 v[244:247], v44, %[rs], s62 offen offset:1024 sc1\n\t"
      "buffer_load_dwordx4 v[248:251], v43, %[rs], s62 offen offset:2048 sc1\n\t"
      "buffer_load_dwordx4 v[252:255], v44, %[rs], s62 offen offset:2048 sc1\n\t"
      "buffer_load_dwordx4 a[116:119], %[wv], %[rs], s61 offen offset:1024 sc1\n\t"
      "s_waitcnt vmcnt(30)\n\t"
      "v_max3_u32 v49, v67, v64, v65\n\t"
      "v_max_u32 v49, v49, v66\n\t"
      "v_max3_u32 v49, v49, v68, v69\n\t"
      "v_max3_u32 v49, v49, v70, v71\n\t"
      "v_max3_u32 v49, v49, v72, v73\n\t"
      "v_max3_u32 v49, v49, v74, v75\n\t"
      "v_max3_u32 v49, v49, v76, v77\n\t"
      "v_max3_u32 v49, v49, v78, v79\n\t"
      "v_max3_u32 v49, v49, v80, v81\n\t"
      "v_max3_u32 v49, v49, v82, v83\n\t"
      "v_max3_u32 v49, v49, v84, v85\n\t"
      "v_max3_u32 v49, v49, v86, v87\n\t"
      "v_cmp_eq_u32 vcc, -1, v49\n\t"
      "s_cbranch_vccnz .Ls2_0_%=\n\t"
      ".Lr2_0_%=:\n\t"
      "v_mfma_f32_16x16x32_bf16 v[112:115], a[176:179], v[64:67], 0\n\t"
      "v_mfma_f32_16x16x32_bf16 v[116:119], a[180:183], v[64:67], 0\n\t"
      "v_mfma_f32_16x16x32_bf16 v[120:123], a[184:187], v[64:67], 0\n\t"
      "buffer_load_dwordx4 a[120:123], %[wv], %[rs], s61 offen offset:2048 sc1\n\t"
      "v_mfma_f32_16x16x32_bf16 v[124:127], a[188:191], v[64:67], 0\n\t"
      "v_mfma_f32_16x16x32_bf16 v[128:131], a[192:195], v[64:67], 0\n\t"
      "v_mfma_f32_16x16x32_bf16 v[132:135], a[176:179], v[68:71], 0\n\t"
      "buffer_load_dwordx4 a[124:127], %[wv], %[rs], s61 offen offset:3072 sc1\n\t"
      "v_mfma_f32_16x16x32_bf16 v[136:139], a[180:183], v[68:71], 0\n\t"
      "v_mfma_f32_16x16x32_bf16 v[140:143], a[184:187], v[68:71], 0\n\t"
      "v_mfma_f32_16x16x32_bf16 v[144:147], a[188:191], v[68:71], 0\n\t"
      "s_add_u32 s61, %[w2b], 49152\n\t"
      "v_mfma_f32_16x16x32_bf16 v[148:151], a[192:195], v[68:71], 0\n\t"
      "buffer_load_dwordx4 a[128:131], %[wv], %[rs], s61 offen offset:0 sc1\n\t"
      "v_mfma_f32_16x16x32_bf16 v[112:115], a[196:199], v[72:75], v[112:115]\n\t"
      "v_mfma_f32_16x16x32_bf16 v[116:119], a[200:203], v[72:75], v[116:119]\n\t"
      "v_mfma_f32_16x16x32_bf16 v[120:123], a[204:207], v[72:75], v[120:123]\n\t"
      "buffer_load_dwordx4 a[132:135], %[wv], %[rs], s61 offen offset:1024 sc1\n\t"
      "v_mfma_f32_16x16x32_bf16 v[124:127], a[208:211], v[72:75], v[124:127]\n\t"
      "v_mfma_f32_16x16x32_bf16 v[128:131], a[212:215], v[72:75], v[128:131]\n\t"
      "s_waitcnt vmcnt(28)\n\t"
      "v_max3_u32 v49, v91, v88, v89\n\t"
      "v_max_u32 v49, v49, v90\n\t"
      "v_mfma_f32_16x16x32_bf16 v[132:135], a[196:199], v[76:79], v[132:135]\n\t"
      "v_max3_u32 v49, v49, v92, v93\n\t"
      "v_max3_u32 v49, v49, v94, v95\n\t"
      "v_mfma_f32_16x16x32_bf16 v[136:139], a[200:203], v[76:79], v[136:139]\n\t"
      "v_max3_u32 v49, v49, v96, v97\n\t"
      "v_max3_u32 v49, v49, v98, v99\n\t"
      "v_mfma_f32_16x16x32_bf16 v[140:143], a[204:207], v[76:79], v[140:143]\n\t"
      "v_max3_u32 v49, v49, v100, v101\n\t"
      "v_max3_u32 v49, v49, v102, v103\n\t"
      "v_mfma_f32_16x16x32_bf16 v[144:147], a[208:211], v[76:79], v[144:147]\n\t"
      "v_max3_u32 v49, v49, v104, v105\n\t"
      "v_max3_u32 v49, v49, v106, v107\n\t"
      "v_mfma_f32_16x16x32_bf16 v[148:151], a[212:215], v[76:79], v[148:151]\n\t"
      "v_max3_u32 v49, v49, v108, v109\n\t"
      "v_max3_u32 v49, v49, v110, v111\n\t"
      "v_mfma_f32_16x16x32_bf16 v[112:115], a[216:219], v[80:83], v[112:115]\n\t"
      "buffer_load_dwordx4 a[136:139], %[wv], %[rs], s61 offen offset:2048 sc1\n\t"
      "v_mfma_f32_16x16x32_bf16 v[116:119], a[220:223], v[80:83], v[116:119]\n\t"
      "buffer_load_dwordx4 a[140:143], %[wv], %[rs], s61 offen offset:3072 sc1\n\t"
      "v_mfma_f32_16x16x32_bf16 v[120:123], a[240:243], v[80:83], v[120:123]\n\t"
      "v_mfma_f32_16x16x32_bf16 v[124:127], a[244:247], v[80:83], v[124:127]\n\t"
      "s_add_u32 s61, %[w2b], 53248\n\t"
      "v_mfma_f32_16x16x32_bf16 v[128:131], a[248:251], v[80:83], v[128:131]\n\t"
      "buffer_load_dwordx4 a[144:147], %[wv], %[rs], s61 offen offset:0 sc1\n\t"
      "v_mfma_f32_16x16x32_bf16 v[132:135], a[216:219], v[84:87], v[132:135]\n\t"
      "v_mfma_f32_16x16x32_bf16 v[136:139], a[220:223], v[84:87], v[136:139]\n\t"
      "v_mfma_f32_16x16x32_bf16 v[140:143], a[240:243], v[84:87], v[140:143]\n\t"
      "buffer_load_dwordx4 a[148:151], %[wv], %[rs], s61 offen offset:1024 sc1\n\t"
      "v_mfma_f32_16x16x32_bf16 v[144:147], a[244:247], v[84:87], v[144:147]\n\t"
      "v_cmp_eq_u32 vcc, -1, v49\n\t"
      "v_mfma_f32_16x16x32_bf16 v[148:151], a[248:251], v[84:87], v[148:151]\n\t"
      "s_cbranch_vccnz .Ls2_1_%=\n\t"
      ".Lr2_1_%=:\n\t"
      "v_mfma_f32_16x16x32_bf16 v[112:115], a[252:255], v[88:91], v[112:115]\n\t"
      "buffer_load_dwordx4 a[152:155], %[wv], %[rs], s61 offen offset:2048 sc1\n\t"
      "v_mfma_f32_16x16x32_bf16 v[116:119], a[0:3], v[88:91], v[116:119]\n\t"
      "v_mfma_f32_16x16x32_bf16 v[120:123], a[4:7], v[88:91], v[120:123]\n\t"
      "v_mfma_f32_16x16x32_bf16 v[124:127], a[8:11], v[88:91], v[124:127]\n\t"
      "buffer_load_dwordx4 a[156:159], %[wv], %[rs], s61 offen offset:3072 sc1\n\t"
      "v_mfma_f32_16x16x32_bf16 v[128:131], a[12:15], v[88:91], v[128:131]\n\t"
      "v_mfma_f32_16x16x32_bf16 v[132:135], a[252:255], v[92:95], v[132:135]\n\t"
      "s_add_u32 s61, %[w2b], 57344\n\t"
      "v_mfma_f32_16x16x32_bf16 v[136:139], a[0:3], v[92:95], v[136:139]\n\t"
      "buffer_load_dwordx4 a[160:163], %[wv], %[rs], s61 offen offset:0 sc1\n\t"
      "v_mfma_f32_16x16x32_bf16 v[140:143], a[4:7], v[92:95], v[140:143]\n\t"
      "v_mfma_f32_16x16x32_bf16 v[144:147], a[8:11], v[92:95], v[144:147]\n\t"
      "v_mfma_f32_16x16x32_bf16 v[148:151], a[12:15], v[92:95], v[148:151]\n\t"
      "buffer_load_dwordx4 a[164:167], %[wv], %[rs], s61 offen offset:1024 sc1\n\t"
      "v_mfma_f32_16x16x32_bf16 v[112:115], a[16:19], v[96:99], v[112:115]\n\t"
      "v_mfma_f32_16x16x32_bf16 v[116:119], a[20:23], v[96:99], v[116:119]\n\t"
      "v_mfma_f32_16x16x32_bf16 v[120:123], a[24:27], v[96:99], v[120:123]\n\t"
      "buffer_load_dwordx4 a[168:171], %[wv], %[rs], s61 offen offset:2048 sc1\n\t"
      "v_mfma_f32_16x16x32_bf16 v[124:127], a[28:31], v[96:99], v[124:127]\n\t"
      "v_mfma_f32_16x16x32_bf16 v[128:131], a[32:35], v[96:99], v[128:131]\n\t"
      "s_waitcnt vmcnt(23)\n\t"
      "v_max3_u32 v49, v211, v208, v209\n\t"
      "v_max_u32 v49, v49, v210\n\t"
      "v_mfma_f32_16x16x32_bf16 v[132:135], a[16:19], v[100:103], v[132:135]\n\t"
      "v_max3_u32 v49, v49, v212, v213\n\t"
      "v_max3_u32 v49, v49, v214, v215\n\t"
      "v_mfma_f32_16x16x32_bf16 v[136:139], a[20:23], v[100:103], v[136:139]\n\t"
      "v_max3_u32 v49, v49, v216, v217\n\t"
      "v_max3_u32 v49, v49, v218, v219\n\t"
      "v_mfma_f32_16x16x32_bf16 v[140:143], a[24:27], v[100:103], v[140:143]\n\t"
      "v_max3_u32 v49, v49, v220, v221\n\t"
      "v_max3_u32 v49, v49, v222, v223\n\t"
      "v_mfma_f32_16x16x32_bf16 v[144:147], a[28:31], v[100:103], v[144:147]\n\t"
      "v_max3_u32 v49, v49, v224, v225\n\t"
      "v_max3_u32 v49, v49, v226, v227\n\t"
      "v_mfma_f32_16x16x32_bf16 v[148:151], a[32:35], v[100:103], v[148:151]\n\t"
      "v_max3_u32 v49, v49, v228, v229\n\t"
      "v_max3_u32 v49, v49, v230, v231\n\t"
      "v_mfma_f32_16x16x32_bf16 v[112:115], a[36:39], v[104:107], v[112:115]\n\t"
      "buffer_load_dwordx4 a[172:175], %[wv], %[rs], s61 offen offset:3072 sc1\n\t"
      "v_mfma_f32_16x16x32_bf16 v[116:119], a[40:43], v[104:107], v[116:119]\n\t"
      "s_add_u32 s62, %[hb], 12288\n\t"
      "buffer_load_dwordx4 v[64:67], v43, %[rs], s62 offen offset:0 sc1\n\t"
      "v_mfma_f32_16x16x32_bf16 v[120:123], a[44:47], v[104:107], v[120:123]\n\t"
      "v_mfma_f32_16x16x32_bf16 v[124:127], a[48:51], v[104:107], v[124:127]\n\t"
      "buffer_load_dwordx4 v[68:71], v44, %[rs], s62 offen offset:0 sc1\n\t"
      "v_mfma_f32_16x16x32_bf16 v[128:131], a[52:55], v[104:107], v[128:131]\n\t"
      "v_mfma_f32_16x16x32_bf16 v[132:135], a[36:39], v[108:111], v[132:135]\n\t"
      "v_mfma_f32_16x16x32_bf16 v[136:139], a[40:43], v[108:111], v[136:139]\n\t"
      "buffer_load_dwordx4 v[72:75], v43, %[rs], s62 offen offset:1024 sc1\n\t"
      "v_mfma_f32_16x16x32_bf16 v[140:143], a[44:47], v[108:111], v[140:143]\n\t"
      "v_mfma_f32_16x16x32_bf16 v[144:147], a[48:51], v[108:111], v[144:147]\n\t"
      "v_cmp_eq_u32 vcc, -1, v49\n\t"
      "buffer_load_dwordx4 v[76:79], v44, %[rs], s62 offen offset:1024 sc1\n\t"
      "v_mfma_f32_16x16x32_bf16 v[148:151], a[52:55], v[108:111], v[148:151]\n\t"
      "s_cbranch_vccnz .Ls2_2_%=\n\t"
      ".Lr2_2_%=:\n\t"
      "v_mfma_f32_16x16x32_bf16 v[112:115], a[56:59], v[208:211], v[112:115]\n\t"
      "v_mfma_f32_16x16x32_bf16 v[116:119], a[60:63], v[208:211], v[116:119]\n\t"
      "buffer_load_dwordx4 v[80:83], v43, %[rs], s62 offen offset:2048 sc1\n\t"
      "v_mfma_f32_16x16x32_bf16 v[120:123], a[64:67], v[208:211], v[120:123]\n\t"
      "v_mfma_f32_16x16x32_bf16 v[124:127], a[68:71], v[208:211], v[124:127]\n\t"
      "v_mfma_f32_16x16x32_bf16 v[128:131], a[72:75], v[208:211], v[128:131]\n\t"
      "buffer_load_dwordx4 v[84:87], v44, %[rs], s62 offen offset:2048 sc1\n\t"
      "v_mfma_f32_16x16x32_bf16 v[132:135], a[56:59], v[212:215], v[132:135]\n\t"
      "v_mfma_f32_16x16x32_bf16 v[136:139], a[60:63], v[212:215], v[136:139]\n\t"
      "v_mfma_f32_16x16x32_bf16 v[140:143], a[64:67], v[212:215], v[140:143]\n\t"
      "s_add_u32 s62, %[hb], 36864\n\t"
      "v_mfma_f32_16x16x32_bf16 v[144:147], a[68:71], v[212:215], v[144:147]\n\t"
      "buffer_load_dwordx4 v[88:91], v43, %[rs], s62 offen offset:0 sc1\n\t"
      "v_mfma_f32_16x16x32_bf16 v[148:151], a[72:75], v[212:215], v[148:151]\n\t"
      "v_mfma_f32_16x16x32_bf16 v[112:115], a[76:79], v[216:219], v[112:115]\n\t"
      "v_mfma_f32_16x16x32_bf16 v[116:119], a[80:83], v[216:219], v[116:119]\n\t"
      "v_mfma_f32_16x16x32_bf16 v[120:123], a[84:87], v[216:219], v[120:123]\n\t"
      "v_mfma_f32_16x16x32_bf16 v[124:127], a[88:91], v[216:219], v[124:127]\n\t"
      "buffer_load_dwordx4 v[92:95], v44, %[rs], s62 offen offset:0 sc1\n\t"
      "v_mfma_f32_16x16x32_bf16 v[128:131], a[92:95], v[216:219], v[128:131]\n\t"
      "s_waitcnt vmcnt(23)\n\t"
      "v_max3_u32 v49, v235, v232, v233\n\t"
      "v_max_u32 v49, v49, v234\n\t"
      "v_mfma_f32_16x16x32_bf16 v[132:135], a[76:79], v[220:223], v[132:135]\n\t"
      "v_max3_u32 v49, v49, v236, v237\n\t"
      "v_max3_u32 v49, v49, v238, v239\n\t"
      "v_mfma_f32_16x16x32_bf16 v[136:139], a[80:83], v[220:223], v[136:139]\n\t"
      "v_max3_u32 v49, v49, v240, v241\n\t"
      "v_max3_u32 v49, v49, v242, v243\n\t"
      "v_mfma_f32_16x16x32_bf16 v[140:143], a[84:87], v[220:223], v[140:143]\n\t"
      "v_max3_u32 v49, v49, v244, v245\n\t"
      "v_max3_u32 v49, v49, v246, v247\n\t"
      "v_mfma_f32_16x16x32_bf16 v[144:147], a[88:91], v[220:223], v[144:147]\n\t"
      "v_max3_u32 v49, v49, v248, v249\n\t"
      "v_max3_u32 v49, v49, v250, v251\n\t"
      "v_mfma_f32_16x16x32_bf16 v[148:151], a[92:95], v[220:223], v[148:151]\n\t"
      "v_max3_u32 v49, v49, v252, v253\n\t"
      "v_max3_u32 v49, v49, v254, v255\n\t"
      "v_mfma_f32_16x16x32_bf16 v[112:115], a[96:99], v[224:227], v[112:115]\n\t"
      "buffer_load_dwordx4 v[96:99], v43, %[rs], s62 offen offset:1024 sc1\n\t"
      "v_mfma_f32_16x16x32_bf16 v[116:119], a[100:103], v[224:227], v[116:119]\n\t"
      "buffer_load_dwordx4 v[100:103], v44, %[rs], s62 offen offset:1024 sc1\n\t"
      "v_mfma_f32_16x16x32_bf16 v[120:123], a[104:107], v[224:227], v[120:123]\n\t"
      "v_mfma_f32_16x16x32_bf16 v[124:127], a[108:111], v[224:227], v[124:127]\n\t"
      "v_mfma_f32_16x16x32_bf16 v[128:131], a[112:115], v[224:227], v[128:131]\n\t"
      "v_mfma_f32_16x16x32_bf16 v[132:135], a[96:99], v[228:231], v[132:135]\n\t"
      "v_mfma_f32_16x16x32_bf16 v[136:139], a[100:103], v[228:231], v[136:139]\n\t"
      "buffer_load_dwordx4 v[104:107], v43, %[rs], s62 offen offset:2048 sc1\n\t"
      "v_mfma_f32_16x16x32_bf16 v[140:143], a[104:107], v[228:231], v[140:143]\n\t"
      "v_mfma_f32_16x16x32_bf16 v[144:147], a[108:111], v[228:231], v[144:147]\n\t"
      "v_cmp_eq_u32 vcc, -1, v49\n\t"
      "v_mfma_f32_16x16x32_bf16 v[148:151], a[112:115], v[228:231], v[148:151]\n\t"
      "s_cbranch_vccnz .Ls2_3_%=\n\t"
      ".Lr2_3_%=:\n\t"
      "s_waitcnt vmcnt(11)\n\t"
      "v_mfma_f32_16x16x32_bf16 v[112:115], a[116:119], v[232:235], v[112:115]\n\t"
      "buffer_load_dwordx4 v[108:111], v44, %[rs], s62 offen offset:2048 sc1\n\t"
      "v_mfma_f32_16x16x32_bf16 v[116:119], a[120:123], v[232:235], v[116:119]\n\t"
      "v_mfma_f32_16x16x32_bf16 v[120:123], a[124:127], v[232:235], v[120:123]\n\t"
      "v_mfma_f32_16x16x32_bf16 v[124:127], a[128:131], v[232:235], v[124:127]\n\t"
      "v_mfma_f32_16x16x32_bf16 v[128:131], a[132:135], v[232:235], v[128:131]\n\t"
      "s_add_u32 s62, %[hb], 61440\n\t"
      "v_mfma_f32_16x16x32_bf16 v[132:135], a[116:119], v[236:239], v[132:135]\n\t"
      "buffer_load_dwordx4 v[208:211], v43, %[rs], s62 offen offset:0 sc1\n\t"
      "v_mfma_f32_16x16x32_bf16 v[136:139], a[120:123], v[236:239], v[136:139]\n\t"
      "v_mfma_f32_16x16x32_bf16 v[140:143], a[124:127], v[236:239], v[140:143]\n\t"
      "v_mfma_f32_16x16x32_bf16 v[144:147], a[128:131], v[236:239], v[144:147]\n\t"
      "v_mfma_f32_16x16x32_bf16 v[148:151], a[132:135], v[236:239], v[148:151]\n\t"
      "buffer_load_dwordx4 v[212:215], v44, %[rs], s62 offen offset:0 sc1\n\t"
      "v_mfma_f32_16x16x32_bf16 v[112:115], a[136:139], v[240:243], v[112:115]\n\t"
      "v_mfma_f32_16x16x32_bf16 v[116:119], a[140:143], v[240:243], v[116:119]\n\t"
      "v_mfma_f32_16x16x32_bf16 v[120:123], a[144:147], v[240:243], v[120:123]\n\t"
      "v_mfma_f32_16x16x32_bf16 v[124:127], a[148:151], v[240:243], v[124:127]\n\t"
      "buffer_load_dwordx4 v[216:219], v43, %[rs], s62 offen offset:1024 sc1\n\t"
      "v_mfma_f32_16x16x32_bf16 v[128:131], a[152:155], v[240:243], v[128:131]\n\t"
      "s_waitcnt vmcnt(9)\n\t"
      "v_max3_u32 v49, v67, v64, v65\n\t"
      "v_max_u32 v49, v49, v66\n\t"
      "v_mfma_f32_16x16x32_bf16 v[132:135], a[136:139], v[244:247], v[132:135]\n\t"
      "v_max3_u32 v49, v49, v68, v69\n\t"
      "v_max3_u32 v49, v49, v70, v71\n\t"
      "v_mfma_f32_16x16x32_bf16 v[136:139], a[140:143], v[244:247], v[136:139]\n\t"
      "v_max3_u32 v49, v49, v72, v73\n\t"
      "v_max3_u32 v49, v49, v74, v75\n\t"
      "v_mfma_f32_16x16x32_bf16 v[140:143], a[144:147], v[244:247], v[140:143]\n\t"
      "v_max3_u32 v49, v49, v76, v77\n\t"
      "v_max3_u32 v49, v49, v78, v79\n\t"
      "v_mfma_f32_16x16x32_bf16 v[144:147], a[148:151], v[244:247], v[144:147]\n\t"
      "v_max3_u32 v49, v49, v80, v81\n\t"
      "v_max3_u32 v49, v49, v82, v83\n\t"
      "v_mfma_f32_16x16x32_bf16 v[148:151], a[152:155], v[244:247], v[148:151]\n\t"
      "v_max3_u32 v49, v49, v84, v85\n\t"
      "v_max3_u32 v49, v49, v86, v87\n\t"
      "v_mfma_f32_16x16x32_bf16 v[112:115], a[156:159], v[248:251], v[112:115]\n\t"
      "buffer_load_dwordx4 v[220:223], v44, %[rs], s62 offen offset:1024 sc1\n\t"
      "v_mfma_f32_16x16x32_bf16 v[116:119], a[160:163], v[248:251], v[116:119]\n\t"
      "v_mfma_f32_16x16x32_bf16 v[120:123], a[164:167], v[248:251], v[120:123]\n\t"
      "buffer_load_dwordx4 v[224:227], v43, %[rs], s62 offen offset:2048 sc1\n\t"
      "v_mfma_f32_16x16x32_bf16 v[124:127], a[168:171], v[248:251], v[124:127]\n\t"
      "v_mfma_f32_16x16x32_bf16 v[128:131], a[172:175], v[248:251], v[128:131]\n\t"
      "v_mfma_f32_16x16x32_bf16 v[132:135], a[156:159], v[252:255], v[132:135]\n\t"
      "v_mfma_f32_16x16x32_bf16 v[136:139], a[160:163], v[252:255], v[136:139]\n\t"
      "v_mfma_f32_16x16x32_bf16 v[140:143], a[164:167], v[252:255], v[140:143]\n\t"
      "buffer_load_dwordx4 v[228:231], v44, %[rs], s62 offen offset:2048 sc1\n\t"
      "v_mfma_f32_16x16x32_bf16 v[144:147], a[168:171], v[252:255], v[144:147]\n\t"
      "v_cmp_eq_u32 vcc, -1, v49\n\t"
      "v_mfma_f32_16x16x32_bf16 v[148:151], a[172:175], v[252:255], v[148:151]\n\t"
      "s_cbranch_vccnz .Ls2_4_%=\n\t"
      ".Lr2_4_%=:\n\t"
      "s_barrier\n\t"
      "v_mfma_f32_16x16x32_bf16 v[160:163], a[176:179], v[64:67], 0\n\t"
      "v_mfma_f32_16x16x32_bf16 v[164:167], a[180:183], v[64:67], 0\n\t"
      "buffer_load_dwordx4 v[152:155], %[oo], %[ro], 0 offen sc1\n\t"
      "v_mfma_f32_16x16x32_bf16 v[168:171], a[184:187], v[64:67], 0\n\t"
      "v_mfma_f32_16x16x32_bf16 v[172:175], a[188:191], v[64:67], 0\n\t"
      "v_mfma_f32_16x16x32_bf16 v[176:179], a[192:195], v[64:67], 0\n\t"
      "s_add_u32 s62, %[hb], 86016\n\t"
      "v_mfma_f32_16x16x32_bf16 v[180:183], a[176:179], v[68:71], 0\n\t"
      "buffer_load_dwordx4 v[232:235], v43, %[rs], s62 offen offset:0 sc1\n\t"
      "v_mfma_f32_16x16x32_bf16 v[184:187], a[180:183], v[68:71], 0\n\t"
      "v_mfma_f32_16x16x32_bf16 v[188:191], a[184:187], v[68:71], 0\n\t"
      "v_mfma_f32_16x16x32_bf16 v[192:195], a[188:191], v[68:71], 0\n\t"
      "v_mfma_f32_16x16x32_bf16 v[196:199], a[192:195], v[68:71], 0\n\t"
      "v_mfma_f32_16x16x32_bf16 v[160:163], a[196:199], v[72:75], v[160:163]\n\t"
      "buffer_load_dwordx4 v[236:239], v44, %[rs], s62 offen offset:0 sc1\n\t"
      "v_mfma_f32_16x16x32_bf16 v[164:167], a[200:203], v[72:75], v[164:167]\n\t"
      "v_mfma_f32_16x16x32_bf16 v[168:171], a[204:207], v[72:75], v[168:171]\n\t"
      "ds_write_b128 v47, v[112:115] offset:0\n\t"
      "v_mfma_f32_16x16x32_bf16 v[172:175], a[208:211], v[72:75], v[172:175]\n\t"
      "v_mfma_f32_16x16x32_bf16 v[176:179], a[212:215], v[72:75], v[176:179]\n\t"
      "s_waitcnt vmcnt(9)\n\t"
      "v_max3_u32 v49, v91, v88, v89\n\t"
      "v_max_u32 v49, v49, v90\n\t"
      "v_mfma_f32_16x16x32_bf16 v[180:183], a[196:199], v[76:79], v[180:183]\n\t"
      "v_max3_u32 v49, v49, v92, v93\n\t"
      "v_max3_u32 v49, v49, v94, v95\n\t"
      "v_mfma_f32_16x16x32_bf16 v[184:187], a[200:203], v[76:79], v[184:187]\n\t"
      "v_max3_u32 v49, v49, v96, v97\n\t"
      "v_max3_u32 v49, v49, v98, v99\n\t"
      "v_mfma_f32_16x16x32_bf16 v[188:191], a[204:207], v[76:79], v[188:191]\n\t"
      "v_max3_u32 v49, v49, v100, v101\n\t"
      "v_max3_u32 v49, v49, v102, v103\n\t"
      "v_mfma_f32_16x16x32_bf16 v[192:195], a[208:211], v[76:79], v[192:195]\n\t"
      "v_max3_u32 v49, v49, v104, v105\n\t"
      "v_max3_u32 v49, v49, v106, v107\n\t"
      "v_mfma_f32_16x16x32_bf16 v[196:199], a[212:215], v[76:79], v[196:199]\n\t"
      "v_max3_u32 v49, v49, v108, v109\n\t"
      "v_max3_u32 v49, v49, v110, v111\n\t"
      "v_mfma_f32_16x16x32_bf16 v[160:163], a[216:219], v[80:83], v[160:163]\n\t"
      "ds_write_b128 v47, v[116:119] offset:1024\n\t"
      "buffer_load_dwordx4 v[240:243], v43, %[rs], s62 offen offset:1024 sc1\n\t"
      "v_mfma_f32_16x16x32_bf16 v[164:167], a[220:223], v[80:83], v[164:167]\n\t"
      "buffer_load_dwordx4 v[244:247], v44, %[rs], s62 offen offset:1024 sc1\n\t"
      "v_mfma_f32_16x16x32_bf16 v[168:171], a[240:243], v[80:83], v[168:171]\n\t"
      "v_mfma_f32_16x16x32_bf16 v[172:175], a[244:247], v[80:83], v[172:175]\n\t"
      "v_mfma_f32_16x16x32_bf16 v[176:179], a[248:251], v[80:83], v[176:179]\n\t"
      "v_mfma_f32_16x16x32_bf16 v[180:183], a[216:219], v[84:87], v[180:183]\n\t"
      "ds_write_b128 v47, v[120:123] offset:2048\n\t"
      "buffer_load_dwordx4 v[248:251], v43, %[rs], s62 offen offset:2048 sc1\n\t"
      "v_mfma_f32_16x16x32_bf16 v[184:187], a[220:223], v[84:87], v[184:187]\n\t"
      "v_mfma_f32_16x16x32_bf16 v[188:191], a[240:243], v[84:87], v[188:191]\n\t"
      "v_mfma_f32_16x16x32_bf16 v[192:195], a[244:247], v[84:87], v[192:195]\n\t"
      "v_cmp_eq_u32 vcc, -1, v49\n\t"
      "v_mfma_f32_16x16x32_bf16 v[196:199], a[248:251], v[84:87], v[196:199]\n\t"
      "buffer_load_dwordx4 v[252:255], v44, %[rs], s62 offen offset:2048 sc1\n\t"
      "s_cbranch_vccnz .Ls2_5_%=\n\t"
      ".Lr2_5_%=:\n\t"
      "v_mfma_f32_16x16x32_bf16 v[160:163], a[252:255], v[88:91], v[160:163]\n\t"
      "ds_write_b128 v47, v[124:127] offset:3072\n\t"
      "v_mfma_f32_16x16x32_bf16 v[164:167], a[0:3], v[88:91], v[164:167]\n\t"
      "v_mfma_f32_16x16x32_bf16 v[168:171], a[4:7], v[88:91], v[168:171]\n\t"
      "v_mfma_f32_16x16x32_bf16 v[172:175], a[8:11], v[88:91], v[172:175]\n\t"
      "v_mfma_f32_16x16x32_bf16 v[176:179], a[12:15], v[88:91], v[176:179]\n\t"
      "buffer_load_dwordx4 v[156:159], %[oo], %[ro], 0 offen offset:64 sc1\n\t"
      "v_mfma_f32_16x16x32_bf16 v[180:183], a[252:255], v[92:95], v[180:183]\n\t"
      "ds_write_b128 v47, v[128:131] offset:4096\n\t"
      "v_mfma_f32_16x16x32_bf16 v[184:187], a[0:3], v[92:95], v[184:187]\n\t"
      "v_mfma_f32_16x16x32_bf16 v[188:191], a[4:7], v[92:95], v[188:191]\n\t"
      "v_mfma_f32_16x16x32_bf16 v[192:195], a[8:11], v[92:95], v[192:195]\n\t"
      "buffer_load_dwordx2 v[200:201], v46, %[ro], 0 offen offset:128 sc1\n\t"
      "v_mfma_f32_16x16x32_bf16 v[196:199], a[12:15], v[92:95], v[196:199]\n\t"
      "v_mfma_f32_16x16x32_bf16 v[160:163], a[16:19], v[96:99], v[160:163]\n\t"
      "ds_write_b128 v48, v[132:135] offset:0\n\t"
      "v_mfma_f32_16x16x32_bf16 v[164:167], a[20:23], v[96:99], v[164:167]\n\t"
      "v_mfma_f32_16x16x32_bf16 v[168:171], a[24:27], v[96:99], v[168:171]\n\t"
      "buffer_load_dwordx4 v[64:67], %[gbo], %[rs], 0 offen offset:0 sc1\n\t"
      "v_mfma_f32_16x16x32_bf16 v[172:175], a[28:31], v[96:99], v[172:175]\n\t"
      "v_mfma_f32_16x16x32_bf16 v[176:179], a[32:35], v[96:99], v[176:179]\n\t"
      "s_waitcnt vmcnt(10)\n\t"
      "v_max3_u32 v49, v211, v208, v209\n\t"
      "v_max_u32 v49, v49, v210\n\t"
      "v_mfma_f32_16x16x32_bf16 v[180:183], a[16:19], v[100:103], v[180:183]\n\t"
      "v_max3_u32 v49, v49, v212, v213\n\t"
      "v_max3_u32 v49, v49, v214, v215\n\t"
      "v_mfma_f32_16x16x32_bf16 v[184:187], a[20:23], v[100:103], v[184:187]\n\t"
      "v_max3_u32 v49, v49, v216, v217\n\t"
      "v_max3_u32 v49, v49, v218, v219\n\t"
      "v_mfma_f32_16x16x32_bf16 v[188:191], a[24:27], v[100:103], v[188:191]\n\t"
      "v_max3_u32 v49, v49, v220, v221\n\t"
      "v_max3_u32 v49, v49, v222, v223\n\t"
      "v_mfma_f32_16x16x32_bf16 v[192:195], a[28:31], v[100:103], v[192:195]\n\t"
      "v_max3_u32 v49, v49, v224, v225\n\t"
      "v_max3_u32 v49, v49, v226, v227\n\t"
      "v_mfma_f32_16x16x32_bf16 v[196:199], a[32:35], v[100:103], v[196:199]\n\t"
      "v_max3_u32 v49, v49, v228, v229\n\t"
      "v_max3_u32 v49, v49, v230, v231\n\t"
      "v_mfma_f32_16x16x32_bf16 v[160:163], a[36:39], v[104:107], v[160:163]\n\t"
      "ds_write_b128 v48, v[136:139] offset:1024\n\t"
      "buffer_load_dwordx4 v[68:71], %[gbo], %[rs], 0 offen offset:16 sc1\n\t"
      "v_mfma_f32_16x16x32_bf16 v[164:167], a[40:43], v[104:107], v[164:167]\n\t"
      "buffer_load_dwordx4 v[72:75], %[gbo], %[rs], 0 offen offset:128 sc1\n\t"
      "v_mfma_f32_16x16x32_bf16 v[168:171], a[44:47], v[104:107], v[168:171]\n\t"
      "v_mfma_f32_16x16x32_bf16 v[172:175], a[48:51], v[104:107], v[172:175]\n\t"
      "v_mfma_f32_16x16x32_bf16 v[176:179], a[52:55], v[104:107], v[176:179]\n\t"
      "v_mfma_f32_16x16x32_bf16 v[180:183], a[36:39], v[108:111], v[180:183]\n\t"
      "ds_write_b128 v48, v[140:143] offset:2048\n\t"
      "v_mfma_f32_16x16x32_bf16 v[184:187], a[40:43], v[108:111], v[184:187]\n\t"
      "buffer_load_dwordx4 v[76:79], %[gbo], %[rs], 0 offen offset:144 sc1\n\t"
      "v_mfma_f32_16x16x32_bf16 v[188:191], a[44:47], v[108:111], v[188:191]\n\t"
      "v_mfma_f32_16x16x32_bf16 v[192:195], a[48:51], v[108:111], v[192:195]\n\t"
      "v_cmp_eq_u32 vcc, -1, v49\n\t"
      "v_mfma_f32_16x16x32_bf16 v[196:199], a[52:55], v[108:111], v[196:199]\n\t"
      "s_cbranch_vccnz .Ls2_6_%=\n\t"
      ".Lr2_6_%=:\n\t"
      "v_mfma_f32_16x16x32_bf16 v[160:163], a[56:59], v[208:211], v[160:163]\n\t"
      "ds_write_b128 v48, v[144:147] offset:3072\n\t"
      "buffer_load_dwordx4 v[80:83], v32, %[rs], 0 offen offset:256 sc1\n\t"
      "v_mfma_f32_16x16x32_bf16 v[164:167], a[60:63], v[208:211], v[164:167]\n\t"
      "v_mfma_f32_16x16x32_bf16 v[168:171], a[64:67], v[208:211], v[168:171]\n\t"
      "v_mfma_f32_16x16x32_bf16 v[172:175], a[68:71], v[208:211], v[172:175]\n\t"
      "v_mfma_f32_16x16x32_bf16 v[176:179], a[72:75], v[208:211], v[176:179]\n\t"
      "v_mfma_f32_16x16x32_bf16 v[180:183], a[56:59], v[212:215], v[180:183]\n\t"
      "ds_write_b128 v48, v[148:151] offset:4096\n\t"
      "v_mfma_f32_16x16x32_bf16 v[184:187], a[60:63], v[212:215], v[184:187]\n\t"
      "v_mfma_f32_16x16x32_bf16 v[188:191], a[64:67], v[212:215], v[188:191]\n\t"
      "v_mfma_f32_16x16x32_bf16 v[192:195], a[68:71], v[212:215], v[192:195]\n\t"
      "v_mfma_f32_16x16x32_bf16 v[196:199], a[72:75], v[212:215], v[196:199]\n\t"
      "v_mfma_f32_16x16x32_bf16 v[160:163], a[76:79], v[216:219], v[160:163]\n\t"
      "v_mfma_f32_16x16x32_bf16 v[164:167], a[80:83], v[216:219], v[164:167]\n\t"
      "v_mfma_f32_16x16x32_bf16 v[168:171], a[84:87], v[216:219], v[168:171]\n\t"
      "v_mfma_f32_16x16x32_bf16 v[172:175], a[88:91], v[216:219], v[172:175]\n\t"
      "v_mfma_f32_16x16x32_bf16 v[176:179], a[92:95], v[216:219], v[176:179]\n\t"
      "s_waitcnt vmcnt(7)\n\t"
      "v_max3_u32 v49, v235, v232, v233\n\t"
      "v_max_u32 v49, v49, v234\n\t"
      "v_mfma_f32_16x16x32_bf16 v[180:183], a[76:79], v[220:223], v[180:183]\n\t"
      "v_max3_u32 v49, v49, v236, v237\n\t"
      "v_max3_u32 v49, v49, v238, v239\n\t"
      "v_mfma_f32_16x16x32_bf16 v[184:187], a[80:83], v[220:223], v[184:187]\n\t"
      "v_max3_u32 v49, v49, v240, v241\n\t"
      "v_max3_u32 v49, v49, v242, v243\n\t"
      "v_mfma_f32_16x16x32_bf16 v[188:191], a[84:87], v[220:223], v[188:191]\n\t"
      "v_max3_u32 v49, v49, v244, v245\n\t"
      "v_max3_u32 v49, v49, v246, v247\n\t"
      "v_mfma_f32_16x16x32_bf16 v[192:195], a[88:91], v[220:223], v[192:195]\n\t"
      "v_max3_u32 v49, v49, v248, v249\n\t"
      "v_max3_u32 v49, v49, v250, v251\n\t"
      "v_mfma_f32_16x16x32_bf16 v[196:199], a[92:95], v[220:223], v[196:199]\n\t"
      "v_max3_u32 v49, v49, v252, v253\n\t"
      "v_max3_u32 v49, v49, v254, v255\n\t"
      "v_mfma_f32_16x16x32_bf16 v[160:163], a[96:99], v[224:227], v[160:163]\n\t"
      "v_mfma_f32_16x16x32_bf16 v[164:167], a[100:103], v[224:227], v[164:167]\n\t"
      "v_mfma_f32_16x16x32_bf16 v[168:171], a[104:107], v[224:227], v[168:171]\n\t"
      "v_mfma_f32_16x16x32_bf16 v[172:175], a[108:111], v[224:227], v[172:175]\n\t"
      "v_mfma_f32_16x16x32_bf16 v[176:179], a[112:115], v[224:227], v[176:179]\n\t"
      "v_mfma_f32_16x16x32_bf16 v[180:183], a[96:99], v[228:231], v[180:183]\n\t"
      "v_mfma_f32_16x16x32_bf16 v[184:187], a[100:103], v[228:231], v[184:187]\n\t"
      "v_mfma_f32_16x16x32_bf16 v[188:191], a[104:107], v[228:231], v[188:191]\n\t"
      "v_mfma_f32_16x16x32_bf16 v[192:195], a[108:111], v[228:231], v[192:195]\n\t"
      "v_cmp_eq_u32 vcc, -1, v49\n\t"
      "v_mfma_f32_16x16x32_bf16 v[196:199], a[112:115], v[228:231], v[196:199]\n\t"
      "s_cbranch_vccnz .Ls2_7_%=\n\t"
      ".Lr2_7_%=:\n\t"
      "v_mfma_f32_16x16x32_bf16 v[180:183], a[116:119], v[236:239], v[180:183]\n\t"
      "v_mfma_f32_16x16x32_bf16 v[184:187], a[120:123], v[236:239], v[184:187]\n\t"
      "v_mfma_f32_16x16x32_bf16 v[188:191], a[124:127], v[236:239], v[188:191]\n\t"
      "v_mfma_f32_16x16x32_bf16 v[192:195], a[128:131], v[236:239], v[192:195]\n\t"
      "v_mfma_f32_16x16x32_bf16 v[196:199], a[132:135], v[236:239], v[196:199]\n\t"
      "v_mfma_f32_16x16x32_bf16 v[180:183], a[136:139], v[244:247], v[180:183]\n\t"
      "v_mfma_f32_16x16x32_bf16 v[184:187], a[140:143], v[244:247], v[184:187]\n\t"
      "v_mfma_f32_16x16x32_bf16 v[188:191], a[144:147], v[244:247], v[188:191]\n\t"
      "v_mfma_f32_16x16x32_bf16 v[192:195], a[148:151], v[244:247], v[192:195]\n\t"
      "v_mfma_f32_16x16x32_bf16 v[196:199], a[152:155], v[244:247], v[196:199]\n\t"
      "v_mfma_f32_16x16x32_bf16 v[180:183], a[156:159], v[252:255], v[180:183]\n\t"
      "v_mfma_f32_16x16x32_bf16 v[184:187], a[160:163], v[252:255], v[184:187]\n\t"
      "v_mfma_f32_16x16x32_bf16 v[188:191], a[164:167], v[252:255], v[188:191]\n\t"
      "v_mfma_f32_16x16x32_bf16 v[192:195], a[168:171], v[252:255], v[192:195]\n\t"
      "v_mfma_f32_16x16x32_bf16 v[196:199], a[172:175], v[252:255], v[196:199]\n\t"
      "v_mfma_f32_16x16x32_bf16 v[160:163], a[116:119], v[232:235], v[160:163]\n\t"
      "v_mfma_f32_16x16x32_bf16 v[164:167], a[120:123], v[232:235], v[164:167]\n\t"
      "v_mfma_f32_16x16x32_bf16 v[168:171], a[124:127], v[232:235], v[168:171]\n\t"
      "v_mfma_f32_16x16x32_bf16 v[172:175], a[128:131], v[232:235], v[172:175]\n\t"
      "v_mfma_f32_16x16x32_bf16 v[176:179], a[132:135], v[232:235], v[176:179]\n\t"
      "v_mfma_f32_16x16x32_bf16 v[160:163], a[136:139], v[240:243], v[160:163]\n\t"
      "v_mfma_f32_16x16x32_bf16 v[164:167], a[140:143], v[240:243], v[164:167]\n\t"
      "v_mfma_f32_16x16x32_bf16 v[168:171], a[144:147], v[240:243], v[168:171]\n\t"
      "v_mfma_f32_16x16x32_bf16 v[172:175], a[148:151], v[240:243], v[172:175]\n\t"
      "ds_write_b128 v48, v[180:183] offset:40960\n\t"
      "v_mfma_f32_16x16x32_bf16 v[176:179], a[152:155], v[240:243], v[176:179]\n\t"
      "ds_write_b128 v48, v[184:187] offset:41984\n\t"
      "v_mfma_f32_16x16x32_bf16 v[160:163], a[156:159], v[248:251], v[160:163]\n\t"
      "ds_write_b128 v48, v[188:191] offset:43008\n\t"
      "v_mfma_f32_16x16x32_bf16 v[164:167], a[160:163], v[248:251], v[164:167]\n\t"
      "ds_write_b128 v48, v[192:195] offset:44032\n\t"
      "v_mfma_f32_16x16x32_bf16 v[168:171], a[164:167], v[248:251], v[168:171]\n\t"
      "ds_write_b128 v48, v[196:199] offset:45056\n\t"
      "v_mfma_f32_16x16x32_bf16 v[172:175], a[168:171], v[248:251], v[172:175]\n\t"
      "v_mfma_f32_16x16x32_bf16 v[176:179], a[172:175], v[248:251], v[176:179]\n\t"
      "s_nop 7\n\t"
      "s_cmp_gt_u32 %[w], 1\n\t"
      "s_cbranch_scc1 .Lown_%=\n\t"
      "ds_write_b128 v47, v[160:163] offset:40960\n\t"
      "ds_write_b128 v47, v[164:167] offset:41984\n\t"
      "ds_write_b128 v47, v[168:171] offset:43008\n\t"
      "ds_write_b128 v47, v[172:175] offset:44032\n\t"
      "ds_write_b128 v47, v[176:179] offset:45056\n\t"
      "ds_read_b128 v[160:163], v47 offset:0\n\t"
      "ds_read_b128 v[164:167], v47 offset:1024\n\t"
      "ds_read_b128 v[168:171], v47 offset:2048\n\t"
      "ds_read_b128 v[172:175], v47 offset:3072\n\t"
      "ds_read_b128 v[176:179], v47 offset:4096\n\t"
      ".Lown_%=:\n\t"
      "s_add_u32 s63, %[w], 1\n\t"
      "s_and_b32 s63, s63, 3\n\t"
      "s_mul_i32 s63, s63, 5120\n\t"
      "s_mul_i32 s62, %[w], 20480\n\t"
      "s_add_u32 s63, s63, s62\n\t"
      "v_add_u32 v33, s63, %[wv]\n\t"
      "s_add_u32 s63, %[w], 2\n\t"
      "s_and_b32 s63, s63, 3\n\t"
      "s_mul_i32 s63, s63, 5120\n\t"
      "s_mul_i32 s62, %[w], 20480\n\t"
      "s_add_u32 s63, s63, s62\n\t"
      "v_add_u32 v34, s63, %[wv]\n\t"
      "s_add_u32 s63, %[w], 3\n\t"
      "s_and_b32 s63, s63, 3\n\t"
      "s_mul_i32 s63, s63, 5120\n\t"
      "s_mul_i32 s62, %[w], 20480\n\t"
      "s_add_u32 s63, s63, s62\n\t"
      "v_add_u32 v35, s63, %[wv]\n\t"
      "s_waitcnt vmcnt(0)\n\t"
      "v_lshlrev_b32 v208, 16, v152\n\t"
      "v_and_b32 v209, 0xffff0000, v152\n\t"
      "v_lshlrev_b32 v88, 16, v64\n\t"
      "v_and_b32 v89, 0xffff0000, v64\n\t"
      "v_lshlrev_b32 v210, 16, v153\n\t"
      "v_and_b32 v211, 0xffff0000, v153\n\t"
      "v_lshlrev_b32 v90, 16, v66\n\t"
      "v_and_b32 v91, 0xffff0000, v66\n\t"
      "v_lshlrev_b32 v212, 16, v154\n\t"
      "v_and_b32 v213, 0xffff0000, v154\n\t"
      "v_lshlrev_b32 v92, 16, v68\n\t"
      "v_and_b32 v93, 0xffff0000, v68\n\t"
      "v_lshlrev_b32 v214, 16, v155\n\t"
      "v_and_b32 v215, 0xffff0000, v155\n\t"
      "v_lshlrev_b32 v94, 16, v70\n\t"
      "v_and_b32 v95, 0xffff0000, v70\n\t"
      "v_lshlrev_b32 v216, 16, v156\n\t"
      "v_and_b32 v217, 0xffff0000, v156\n\t"
      "v_lshlrev_b32 v96, 16, v72\n\t"
      "v_and_b32 v97, 0xffff0000, v72\n\t"
      "v_lshlrev_b32 v218, 16, v157\n\t"
      "v_and_b32 v219, 0xffff0000, v157\n\t"
      "v_lshlrev_b32 v98, 16, v74\n\t"
      "v_and_b32 v99, 0xffff0000, v74\n\t"
      "v_lshlrev_b32 v220, 16, v158\n\t"
      "v_and_b32 v221, 0xffff0000, v158\n\t"
      "v_lshlrev_b32 v100, 16, v76\n\t"
      "v_and_b32 v101, 0xffff0000, v76\n\t"
      "v_lshlrev_b32 v222, 16, v159\n\t"
      "v_and_b32 v223, 0xffff0000, v159\n\t"
      "v_lshlrev_b32 v102, 16, v78\n\t"
      "v_and_b32 v103, 0xffff0000, v78\n\t"
      "v_lshlrev_b32 v224, 16, v200\n\t"
      "v_and_b32 v225, 0xffff0000, v200\n\t"
      "v_lshlrev_b32 v104, 16, v80\n\t"
      "v_and_b32 v105, 0xffff0000, v80\n\t"
      "v_lshlrev_b32 v226, 16, v201\n\t"
      "v_and_b32 v227, 0xffff0000, v201\n\t"
      "v_lshlrev_b32 v106, 16, v82\n\t"
      "v_and_b32 v107, 0xffff0000, v82\n\t"
      "s_waitcnt lgkmcnt(0)\n\t"
      "s_barrier\n\t"
      "ds_read_b128 v[228:231], v33 offset:0\n\t"
      "ds_read_b128 v[232:235], v33 offset:1024\n\t"
      "ds_read_b128 v[236:239], v33 offset:2048\n\t"
      "ds_read_b128 v[240:243], v33 offset:3072\n\t"
      "ds_read_b128 v[244:247], v33 offset:4096\n\t"
      "ds_read_b128 v[112:115], v34 offset:0\n\t"
      "ds_read_b128 v[116:119], v34 offset:1024\n\t"
      "ds_read_b128 v[120:123], v34 offset:2048\n\t"
      "ds_read_b128 v[124:127], v34 offset:3072\n\t"
      "ds_read_b128 v[128:131], v34 offset:4096\n\t"
      "ds_read_b128 v[132:135], v35 offset:0\n\t"
      "ds_read_b128 v[136:139], v35 offset:1024\n\t"
      "ds_read_b128 v[140:143], v35 offset:2048\n\t"
      "ds_read_b128 v[144:147], v35 offset:3072\n\t"
      "ds_read_b128 v[148:151], v35 offset:4096\n\t"
      "s_waitcnt lgkmcnt(4)\n\t"
      "v_pk_add_f32 v[160:161], v[160:161], v[228:229]\n\t"
      "v_pk_add_f32 v[112:113], v[112:113], v[132:133]\n\t"
      "v_pk_add_f32 v[162:163], v[162:163], v[230:231]\n\t"
      "v_pk_add_f32 v[114:115], v[114:115], v[134:135]\n\t"
      "v_pk_add_f32 v[160:161], v[160:161], v[112:113]\n\t"
      "v_pk_add_f32 v[162:163], v[162:163], v[114:115]\n\t"
      "v_pk_fma_f32 v[160:161], v[160:161], v[88:89], v[208:209]\n\t"
      "v_pk_fma_f32 v[162:163], v[162:163], v[90:91], v[210:211]\n\t"
      "v_cvt_pk_bf16_f32 v64, v160, v161\n\t"
      "v_cvt_pk_bf16_f32 v65, v162, v163\n\t"
      "s_waitcnt lgkmcnt(3)\n\t"
      "v_pk_add_f32 v[164:165], v[164:165], v[232:233]\n\t"
      "v_pk_add_f32 v[116:117], v[116:117], v[136:137]\n\t"
      "v_pk_add_f32 v[166:167], v[166:167], v[234:235]\n\t"
      "v_pk_add_f32 v[118:119], v[118:119], v[138:139]\n\t"
      "v_pk_add_f32 v[164:165], v[164:165], v[116:117]\n\t"
      "v_pk_add_f32 v[166:167], v[166:167], v[118:119]\n\t"
      "v_pk_fma_f32 v[164:165], v[164:165], v[92:93], v[212:213]\n\t"
      "v_pk_fma_f32 v[166:167], v[166:167], v[94:95], v[214:215]\n\t"
      "v_cvt_pk_bf16_f32 v66, v164, v165\n\t"
      "v_cvt_pk_bf16_f32 v67, v166, v167\n\t"
      "s_cmp_lg_u32 %[fail], 0\n\t"
      "s_cbranch_scc1 .Lok_%=\n\t"
      "v_mov_b32 v64, 0x7fc07fc0\n\t"
      ".Lok_%=:\n\t"
      "buffer_store_dwordx4 v[64:67], %[oo], %[ro], 0 offen sc1\n\t"
      "s_waitcnt lgkmcnt(2)\n\t"
      "v_pk_add_f32 v[168:169], v[168:169], v[236:237]\n\t"
      "v_pk_add_f32 v[120:121], v[120:121], v[140:141]\n\t"
      "v_pk_add_f32 v[170:171], v[170:171], v[238:239]\n\t"
      "v_pk_add_f32 v[122:123], v[122:123], v[142:143]\n\t"
      "v_pk_add_f32 v[168:169], v[168:169], v[120:121]\n\t"
      "v_pk_add_f32 v[170:171], v[170:171], v[122:123]\n\t"
      "v_pk_fma_f32 v[168:169], v[168:169], v[96:97], v[216:217]\n\t"
      "v_pk_fma_f32 v[170:171], v[170:171], v[98:99], v[218:219]\n\t"
      "v_cvt_pk_bf16_f32 v68, v168, v169\n\t"
      "v_cvt_pk_bf16_f32 v69, v170, v171\n\t"
      "s_waitcnt lgkmcnt(1)\n\t"
      "v_pk_add_f32 v[172:173], v[172:173], v[240:241]\n\t"
      "v_pk_add_f32 v[124:125], v[124:125], v[144:145]\n\t"
      "v_pk_add_f32 v[174:175], v[174:175], v[242:243]\n\t"
      "v_pk_add_f32 v[126:127], v[126:127], v[146:147]\n\t"
      "v_pk_add_f32 v[172:173], v[172:173], v[124:125]\n\t"
      "v_pk_add_f32 v[174:175], v[174:175], v[126:127]\n\t"
      "v_pk_fma_f32 v[172:173], v[172:173], v[100:101], v[220:221]\n\t"
      "v_pk_fma_f32 v[174:175], v[174:175], v[102:103], v[222:223]\n\t"
      "v_cvt_pk_bf16_f32 v70, v172, v173\n\t"
      "v_cvt_pk_bf16_f32 v71, v174, v175\n\t"
      "buffer_store_dwordx4 v[68:71], %[oo], %[ro], 0 offen offset:64 sc1\n\t"
      "s_waitcnt lgkmcnt(0)\n\t"
      "v_pk_add_f32 v[176:177], v[176:177], v[244:245]\n\t"
      "v_pk_add_f32 v[128:129], v[128:129], v[148:149]\n\t"
      "v_pk_add_f32 v[178:179], v[178:179], v[246:247]\n\t"
      "v_pk_add_f32 v[130:131], v[130:131], v[150:151]\n\t"
      "v_pk_add_f32 v[176:177], v[176:177], v[128:129]\n\t"
      "v_pk_add_f32 v[178:179], v[178:179], v[130:131]\n\t"
      "v_pk_fma_f32 v[176:177], v[176:177], v[104:105], v[224:225]\n\t"
      "v_pk_fma_f32 v[178:179], v[178:179], v[106:107], v[226:227]\n\t"
      "v_cvt_pk_bf16_f32 v72, v176, v177\n\t"
      "v_cvt_pk_bf16_f32 v73, v178, v179\n\t"
      "buffer_store_dwordx2 v[72:73], v46, %[ro], 0 offen offset:128 sc1\n\t"
      "s_branch .Ldone_%=\n\t"
      ".Ls1_g2_%=:\n\t"
      "s_mov_b32 s67, m0\n\t"
      "s_mov_b32 s65, 0\n\t"
      ".Ls1l_g2_%=:\n\t"
      "s_add_u32 s65, s65, 1\n\t"
      "s_cmp_gt_u32 s65, %[fail]\n\t"
      "s_cbranch_scc1 .Lf1_g2_%=\n\t"
      "s_add_u32 m0, s64, 65536\n\t"
      "s_nop 0\n\t"
      "buffer_load_dwordx4 v42, %[rs], m0 offen sc1 lds\n\t"
      "buffer_load_dwordx4 v42, %[rs], m0 offen offset:1024 sc1 lds\n\t"
      "s_add_u32 m0, s64, 81920\n\t"
      "s_nop 0\n\t"
      "buffer_load_dwordx4 v42, %[rs], m0 offen sc1 lds\n\t"
      "buffer_load_dwordx4 v42, %[rs], m0 offen offset:1024 sc1 lds\n\t"
      "s_add_u32 m0, s64, 98304\n\t"
      "s_nop 0\n\t"
      "buffer_load_dwordx4 v42, %[rs], m0 offen sc1 lds\n\t"
      "buffer_load_dwordx4 v42, %[rs], m0 offen offset:1024 sc1 lds\n\t"
      "s_add_u32 m0, s64, 114688\n\t"
      "s_nop 0\n\t"
      "buffer_load_dwordx4 v42, %[rs], m0 offen sc1 lds\n\t"
      "buffer_load_dwordx4 v42, %[rs], m0 offen offset:1024 sc1 lds\n\t"
      "s_add_u32 m0, s64, 131072\n\t"
      "s_nop 0\n\t"
      "buffer_load_dwordx4 v42, %[rs], m0 offen sc1 lds\n\t"
      "buffer_load_dwordx4 v42, %[rs], m0 offen offset:1024 sc1 lds\n\t"
      "s_add_u32 m0, s64, 147456\n\t"
      "s_nop 0\n\t"
      "buffer_load_dwordx4 v42, %[rs], m0 offen sc1 lds\n\t"
      "buffer_load_dwordx4 v42, %[rs], m0 offen offset:1024 sc1 lds\n\t"
      "s_waitcnt vmcnt(0)\n\t"
      "ds_read_b128 v[112:115], v40 offset:0\n\t"
      "ds_read_b128 v[116:119], v40 offset:1024\n\t"
      "ds_read_b128 v[120:123], v40 offset:16384\n\t"
      "ds_read_b128 v[124:127], v40 offset:17408\n\t"
      "ds_read_b128 v[128:131], v40 offset:32768\n\t"
      "ds_read_b128 v[132:135], v40 offset:33792\n\t"
      "ds_read_b128 v[136:139], v40 offset:49152\n\t"
      "ds_read_b128 v[140:143], v40 offset:50176\n\t"
      "ds_read_b128 v[144:147], v41 offset:0\n\t"
      "ds_read_b128 v[148:151], v41 offset:1024\n\t"
      "ds_read_b128 v[152:155], v41 offset:16384\n\t"
      "ds_read_b128 v[156:159], v41 offset:17408\n\t"
      "s_waitcnt lgkmcnt(0)\n\t"
      "v_max3_u32 v49, v115, v112, v113\n\t"
      "v_max_u32 v49, v49, v114\n\t"
      "v_max3_u32 v49, v49, v116, v117\n\t"
      "v_max3_u32 v49, v49, v118, v119\n\t"
      "v_max3_u32 v49, v49, v120, v121\n\t"
      "v_max3_u32 v49, v49, v122, v123\n\t"
      "v_max3_u32 v49, v49, v124, v125\n\t"
      "v_max3_u32 v49, v49, v126, v127\n\t"
      "v_max3_u32 v49, v49, v128, v129\n\t"
      "v_max3_u32 v49, v49, v130, v131\n\t"
      "v_max3_u32 v49, v49, v132, v133\n\t"
      "v_max3_u32 v49, v49, v134, v135\n\t"
      "v_max3_u32 v49, v49, v136, v137\n\t"
      "v_max3_u32 v49, v49, v138, v139\n\t"
      "v_max3_u32 v49, v49, v140, v141\n\t"
      "v_max3_u32 v49, v49, v142, v143\n\t"
      "v_max3_u32 v49, v49, v144, v145\n\t"
      "v_max3_u32 v49, v49, v146, v147\n\t"
      "v_max3_u32 v49, v49, v148, v149\n\t"
      "v_max3_u32 v49, v49, v150, v151\n\t"
      "v_max3_u32 v49, v49, v152, v153\n\t"
      "v_max3_u32 v49, v49, v154, v155\n\t"
      "v_max3_u32 v49, v49, v156, v157\n\t"
      "v_max3_u32 v49, v49, v158, v159\n\t"
      "v_cmp_eq_u32 vcc, -1, v49\n\t"
      "s_cbranch_vccnz .Ls1l_g2_%=\n\t"
      "s_mov_b32 m0, s67\n\t"
      "s_branch .Lr1_g2_%=\n\t"
      ".Lf1_g2_%=:\n\t"
      "s_mov_b32 %[fail], 0\n\t"
      "s_mov_b32 m0, s67\n\t"
      "s_branch .Lr1_g2_%=\n\t"
      ".Ls1_g5_%=:\n\t"
      "s_mov_b32 s67, m0\n\t"
      "s_mov_b32 s65, 0\n\t"
      ".Ls1l_g5_%=:\n\t"
      "s_add_u32 s65, s65, 1\n\t"
      "s_cmp_gt_u32 s65, %[fail]\n\t"
      "s_cbranch_scc1 .Lf1_g5_%=\n\t"
      "s_add_u32 m0, s64, 8192\n\t"
      "s_nop 0\n\t"
      "buffer_load_dwordx4 v42, %[rs], m0 offen sc1 lds\n\t"
      "buffer_load_dwordx4 v42, %[rs], m0 offen offset:1024 sc1 lds\n\t"
      "s_add_u32 m0, s64, 24576\n\t"
      "s_nop 0\n\t"
      "buffer_load_dwordx4 v42, %[rs], m0 offen sc1 lds\n\t"
      "buffer_load_dwordx4 v42, %[rs], m0 offen offset:1024 sc1 lds\n\t"
      "s_add_u32 m0, s64, 40960\n\t"
      "s_nop 0\n\t"
      "buffer_load_dwordx4 v42, %[rs], m0 offen sc1 lds\n\t"
      "buffer_load_dwordx4 v42, %[rs], m0 offen offset:1024 sc1 lds\n\t"
      "s_add_u32 m0, s64, 57344\n\t"
      "s_nop 0\n\t"
      "buffer_load_dwordx4 v42, %[rs], m0 offen sc1 lds\n\t"
      "buffer_load_dwordx4 v42, %[rs], m0 offen offset:1024 sc1 lds\n\t"
      "s_add_u32 m0, s64, 73728\n\t"
      "s_nop 0\n\t"
      "buffer_load_dwordx4 v42, %[rs], m0 offen sc1 lds\n\t"
      "buffer_load_dwordx4 v42, %[rs], m0 offen offset:1024 sc1 lds\n\t"
      "s_add_u32 m0, s64, 90112\n\t"
      "s_nop 0\n\t"
      "buffer_load_dwordx4 v42, %[rs], m0 offen sc1 lds\n\t"
      "buffer_load_dwordx4 v42, %[rs], m0 offen offset:1024 sc1 lds\n\t"
      "s_waitcnt vmcnt(0)\n\t"
      "ds_read_b128 v[112:115], v39 offset:8192\n\t"
      "ds_read_b128 v[116:119], v39 offset:9216\n\t"
      "ds_read_b128 v[120:123], v39 offset:24576\n\t"
      "ds_read_b128 v[124:127], v39 offset:25600\n\t"
      "ds_read_b128 v[128:131], v39 offset:40960\n\t"
      "ds_read_b128 v[132:135], v39 offset:41984\n\t"
      "ds_read_b128 v[136:139], v39 offset:57344\n\t"
      "ds_read_b128 v[140:143], v39 offset:58368\n\t"
      "ds_read_b128 v[144:147], v40 offset:8192\n\t"
      "ds_read_b128 v[148:151], v40 offset:9216\n\t"
      "ds_read_b128 v[152:155], v40 offset:24576\n\t"
      "ds_read_b128 v[156:159], v40 offset:25600\n\t"
      "s_waitcnt lgkmcnt(0)\n\t"
      "v_max3_u32 v49, v115, v112, v113\n\t"
      "v_max_u32 v49, v49, v114\n\t"
      "v_max3_u32 v49, v49, v116, v117\n\t"
      "v_max3_u32 v49, v49, v118, v119\n\t"
      "v_max3_u32 v49, v49, v120, v121\n\t"
      "v_max3_u32 v49, v49, v122, v123\n\t"
      "v_max3_u32 v49, v49, v124, v125\n\t"
      "v_max3_u32 v49, v49, v126, v127\n\t"
      "v_max3_u32 v49, v49, v128, v129\n\t"
      "v_max3_u32 v49, v49, v130, v131\n\t"
      "v_max3_u32 v49, v49, v132, v133\n\t"
      "v_max3_u32 v49, v49, v134, v135\n\t"
      "v_max3_u32 v49, v49, v136, v137\n\t"
      "v_max3_u32 v49, v49, v138, v139\n\t"
      "v_max3_u32 v49, v49, v140, v141\n\t"
      "v_max3_u32 v49, v49, v142, v143\n\t"
      "v_max3_u32 v49, v49, v144, v145\n\t"
      "v_max3_u32 v49, v49, v146, v147\n\t"
      "v_max3_u32 v49, v49, v148, v149\n\t"
      "v_max3_u32 v49, v49, v150, v151\n\t"
      "v_max3_u32 v49, v49, v152, v153\n\t"
      "v_max3_u32 v49, v49, v154, v155\n\t"
      "v_max3_u32 v49, v49, v156, v157\n\t"
      "v_max3_u32 v49, v49, v158, v159\n\t"
      "v_cmp_eq_u32 vcc, -1, v49\n\t"
      "s_cbranch_vccnz .Ls1l_g5_%=\n\t"
      "s_mov_b32 m0, s67\n\t"
      "s_branch .Lr1_g5_%=\n\t"
      ".Lf1_g5_%=:\n\t"
      "s_mov_b32 %[fail], 0\n\t"
      "s_mov_b32 m0, s67\n\t"
      "s_branch .Lr1_g5_%=\n\t"
      ".Ls1_g8_%=:\n\t"
      "s_mov_b32 s67, m0\n\t"
      "s_mov_b32 s65, 0\n\t"
      ".Ls1l_g8_%=:\n\t"
      "s_add_u32 s65, s65, 1\n\t"
      "s_cmp_gt_u32 s65, %[fail]\n\t"
      "s_cbranch_scc1 .Lf1_g8_%=\n\t"
      "s_add_u32 m0, s64, 106496\n\t"
      "s_nop 0\n\t"
      "buffer_load_dwordx4 v42, %[rs], m0 offen sc1 lds\n\t"
      "buffer_load_dwordx4 v42, %[rs], m0 offen offset:1024 sc1 lds\n\t"
      "s_add_u32 m0, s64, 122880\n\t"
      "s_nop 0\n\t"
      "buffer_load_dwordx4 v42, %[rs], m0 offen sc1 lds\n\t"
      "buffer_load_dwordx4 v42, %[rs], m0 offen offset:1024 sc1 lds\n\t"
      "s_add_u32 m0, s64, 139264\n\t"
      "s_nop 0\n\t"
      "buffer_load_dwordx4 v42, %[rs], m0 offen sc1 lds\n\t"
      "buffer_load_dwordx4 v42, %[rs], m0 offen offset:1024 sc1 lds\n\t"
      "s_add_u32 m0, s64, 155648\n\t"
      "s_nop 0\n\t"
      "buffer_load_dwordx4 v42, %[rs], m0 offen sc1 lds\n\t"
      "buffer_load_dwordx4 v42, %[rs], m0 offen offset:1024 sc1 lds\n\t"
      "s_waitcnt vmcnt(0)\n\t"
      "ds_read_b128 v[64:67], v40 offset:40960\n\t"
      "ds_read_b128 v[68:71], v40 offset:41984\n\t"
      "ds_read_b128 v[72:75], v40 offset:57344\n\t"
      "ds_read_b128 v[76:79], v40 offset:58368\n\t"
      "ds_read_b128 v[80:83], v41 offset:8192\n\t"
      "ds_read_b128 v[84:87], v41 offset:9216\n\t"
      "ds_read_b128 v[88:91], v41 offset:24576\n\t"
      "ds_read_b128 v[92:95], v41 offset:25600\n\t"
      "s_waitcnt lgkmcnt(0)\n\t"
      "v_max3_u32 v49, v67, v64, v65\n\t"
      "v_max_u32 v49, v49, v66\n\t"
      "v_max3_u32 v49, v49, v68, v69\n\t"
      "v_max3_u32 v49, v49, v70, v71\n\t"
      "v_max3_u32 v49, v49, v72, v73\n\t"
      "v_max3_u32 v49, v49, v74, v75\n\t"
      "v_max3_u32 v49, v49, v76, v77\n\t"
      "v_max3_u32 v49, v49, v78, v79\n\t"
      "v_max3_u32 v49, v49, v80, v81\n\t"
      "v_max3_u32 v49, v49, v82, v83\n\t"
      "v_max3_u32 v49, v49, v84, v85\n\t"
      "v_max3_u32 v49, v49, v86, v87\n\t"
      "v_max3_u32 v49, v49, v88, v89\n\t"
      "v_max3_u32 v49, v49, v90, v91\n\t"
      "v_max3_u32 v49, v49, v92, v93\n\t"
      "v_max3_u32 v49, v49, v94, v95\n\t"
      "v_cmp_eq_u32 vcc, -1, v49\n\t"
      "s_cbranch_vccnz .Ls1l_g8_%=\n\t"
      "s_mov_b32 m0, s67\n\t"
      "s_branch .Lr1_g8_%=\n\t"
      ".Lf1_g8_%=:\n\t"
      "s_mov_b32 %[fail], 0\n\t"
      "s_mov_b32 m0, s67\n\t"
      "s_branch .Lr1_g8_%=\n\t"
      ".Lsfb_%=:\n\t"
      "s_mov_b32 s65, 0\n\t"
      ".Llfb_%=:\n\t"
      "s_add_u32 s65, s65, 1\n\t"
      "s_cmp_gt_u32 s65, %[fail]\n\t"
      "s_cbranch_scc1 .Lffb_%=\n\t"
      "buffer_load_dwordx4 v[60:63], v52, %[rs], 0 offen sc1\n\t"
      "s_waitcnt vmcnt(0)\n\t"
      "v_xor_b32 v60, %[tag], v60\n\t"
      "v_xor_b32 v61, %[tag], v61\n\t"
      "v_xor_b32 v62, %[tag], v62\n\t"
      "v_xor_b32 v63, %[tag], v63\n\t"
      "v_or3_b32 v60, v60, v61, v62\n\t"
      "v_or_b32 v60, v60, v63\n\t"
      "v_cmp_ne_u32 vcc, 0, v60\n\t"
      "s_cbranch_vccnz .Llfb_%=\n\t"
      "s_branch .Lrfb_%=\n\t"
      ".Lffb_%=:\n\t"
      "s_mov_b32 %[fail], 0\n\t"
      "s_branch .Lrfb_%=\n\t"
      ".Ls2_1_%=:\n\t"
      "s_mov_b32 s65, 0\n\t"
      "s_add_u32 s63, %[hb], 24576\n\t"
      ".Ls2l_1_%=:\n\t"
      "s_add_u32 s65, s65, 1\n\t"
      "s_cmp_gt_u32 s65, %[fail]\n\t"
      "s_cbranch_scc1 .Lf2_1_%=\n\t"
      "buffer_load_dwordx4 v[88:91], v43, %[rs], s63 offen offset:0 sc1\n\t"
      "buffer_load_dwordx4 v[92:95], v44, %[rs], s63 offen offset:0 sc1\n\t"
      "buffer_load_dwordx4 v[96:99], v43, %[rs], s63 offen offset:1024 sc1\n\t"
      "buffer_load_dwordx4 v[100:103], v44, %[rs], s63 offen offset:1024 sc1\n\t"
      "buffer_load_dwordx4 v[104:107], v43, %[rs], s63 offen offset:2048 sc1\n\t"
      "buffer_load_dwordx4 v[108:111], v44, %[rs], s63 offen offset:2048 sc1\n\t"
      "s_waitcnt vmcnt(0)\n\t"
      "v_max3_u32 v49, v91, v88, v89\n\t"
      "v_max_u32 v49, v49, v90\n\t"
      "v_max3_u32 v49, v49, v92, v93\n\t"
      "v_max3_u32 v49, v49, v94, v95\n\t"
      "v_max3_u32 v49, v49, v96, v97\n\t"
      "v_max3_u32 v49, v49, v98, v99\n\t"
      "v_max3_u32 v49, v49, v100, v101\n\t"
      "v_max3_u32 v49, v49, v102, v103\n\t"
      "v_max3_u32 v49, v49, v104, v105\n\t"
      "v_max3_u32 v49, v49, v106, v107\n\t"
      "v_max3_u32 v49, v49, v108, v109\n\t"
      "v_max3_u32 v49, v49, v110, v111\n\t"
      "v_cmp_eq_u32 vcc, -1, v49\n\t"
      "s_cbranch_vccnz .Ls2l_1_%=\n\t"
      "s_branch .Lr2_1_%=\n\t"
      ".Lf2_1_%=:\n\t"
      "s_mov_b32 %[fail], 0\n\t"
      "s_branch .Lr2_1_%=\n\t"
      ".Ls2_2_%=:\n\t"
      "s_mov_b32 s65, 0\n\t"
      "s_add_u32 s63, %[hb], 49152\n\t"
      ".Ls2l_2_%=:\n\t"
      "s_add_u32 s65, s65, 1\n\t"
      "s_cmp_gt_u32 s65, %[fail]\n\t"
      "s_cbranch_scc1 .Lf2_2_%=\n\t"
      "buffer_load_dwordx4 v[208:211], v43, %[rs], s63 offen offset:0 sc1\n\t"
      "buffer_load_dwordx4 v[212:215], v44, %[rs], s63 offen offset:0 sc1\n\t"
      "buffer_load_dwordx4 v[216:219], v43, %[rs], s63 offen offset:1024 sc1\n\t"
      "buffer_load_dwordx4 v[220:223], v44, %[rs], s63 offen offset:1024 sc1\n\t"
      "buffer_load_dwordx4 v[224:227], v43, %[rs], s63 offen offset:2048 sc1\n\t"
      "buffer_load_dwordx4 v[228:231], v44, %[rs], s63 offen offset:2048 sc1\n\t"
      "s_waitcnt vmcnt(0)\n\t"
      "v_max3_u32 v49, v211, v208, v209\n\t"
      "v_max_u32 v49, v49, v210\n\t"
      "v_max3_u32 v49, v49, v212, v213\n\t"
      "v_max3_u32 v49, v49, v214, v215\n\t"
      "v_max3_u32 v49, v49, v216, v217\n\t"
      "v_max3_u32 v49, v49, v218, v219\n\t"
      "v_max3_u32 v49, v49, v220, v221\n\t"
      "v_max3_u32 v49, v49, v222, v223\n\t"
      "v_max3_u32 v49, v49, v224, v225\n\t"
      "v_max3_u32 v49, v49, v226, v227\n\t"
      "v_max3_u32 v49, v49, v228, v229\n\t"
      "v_max3_u32 v49, v49, v230, v231\n\t"
      "v_cmp_eq_u32 vcc, -1, v49\n\t"
      "s_cbranch_vccnz .Ls2l_2_%=\n\t"
      "s_branch .Lr2_2_%=\n\t"
      ".Lf2_2_%=:\n\t"
      "s_mov_b32 %[fail], 0\n\t"
      "s_branch .Lr2_2_%=\n\t"
      ".Ls2_3_%=:\n\t"
      "s_mov_b32 s65, 0\n\t"
      "s_add_u32 s63, %[hb], 73728\n\t"
      ".Ls2l_3_%=:\n\t"
      "s_add_u32 s65, s65, 1\n\t"
      "s_cmp_gt_u32 s65, %[fail]\n\t"
      "s_cbranch_scc1 .Lf2_3_%=\n\t"
      "buffer_load_dwordx4 v[232:235], v43, %[rs], s63 offen offset:0 sc1\n\t"
      "buffer_load_dwordx4 v[236:239], v44, %[rs], s63 offen offset:0 sc1\n\t"
      "buffer_load_dwordx4 v[240:243], v43, %[rs], s63 offen offset:1024 sc1\n\t"
      "buffer_load_dwordx4 v[244:247], v44, %[rs], s63 offen offset:1024 sc1\n\t"
      "buffer_load_dwordx4 v[248:251], v43, %[rs], s63 offen offset:2048 sc1\n\t"
      "buffer_load_dwordx4 v[252:255], v44, %[rs], s63 offen offset:2048 sc1\n\t"
      "s_waitcnt vmcnt(0)\n\t"
      "v_max3_u32 v49, v235, v232, v233\n\t"
      "v_max_u32 v49, v49, v234\n\t"
      "v_max3_u32 v49, v49, v236, v237\n\t"
      "v_max3_u32 v49, v49, v238, v239\n\t"
      "v_max3_u32 v49, v49, v240, v241\n\t"
      "v_max3_u32 v49, v49, v242, v243\n\t"
      "v_max3_u32 v49, v49, v244, v245\n\t"
      "v_max3_u32 v49, v49, v246, v247\n\t"
      "v_max3_u32 v49, v49, v248, v249\n\t"
      "v_max3_u32 v49, v49, v250, v251\n\t"
      "v_max3_u32 v49, v49, v252, v253\n\t"
      "v_max3_u32 v49, v49, v254, v255\n\t"
      "v_cmp_eq_u32 vcc, -1, v49\n\t"
      "s_cbranch_vccnz .Ls2l_3_%=\n\t"
      "s_branch .Lr2_3_%=\n\t"
      ".Lf2_3_%=:\n\t"
      "s_mov_b32 %[fail], 0\n\t"
      "s_branch .Lr2_3_%=\n\t"
      ".Ls2_4_%=:\n\t"
      "s_mov_b32 s65, 0\n\t"
      "s_add_u32 s63, %[hb], 12288\n\t"
      ".Ls2l_4_%=:\n\t"
      "s_add_u32 s65, s65, 1\n\t"
      "s_cmp_gt_u32 s65, %[fail]\n\t"
      "s_cbranch_scc1 .Lf2_4_%=\n\t"
      "buffer_load_dwordx4 v[64:67], v43, %[rs], s63 offen offset:0 sc1\n\t"
      "buffer_load_dwordx4 v[68:71], v44, %[rs], s63 offen offset:0 sc1\n\t"
      "buffer_load_dwordx4 v[72:75], v43, %[rs], s63 offen offset:1024 sc1\n\t"
      "buffer_load_dwordx4 v[76:79], v44, %[rs], s63 offen offset:1024 sc1\n\t"
      "buffer_load_dwordx4 v[80:83], v43, %[rs], s63 offen offset:2048 sc1\n\t"
      "buffer_load_dwordx4 v[84:87], v44, %[rs], s63 offen offset:2048 sc1\n\t"
      "s_waitcnt vmcnt(0)\n\t"
      "v_max3_u32 v49, v67, v64, v65\n\t"
      "v_max_u32 v49, v49, v66\n\t"
      "v_max3_u32 v49, v49, v68, v69\n\t"
      "v_max3_u32 v49, v49, v70, v71\n\t"
      "v_max3_u32 v49, v49, v72, v73\n\t"
      "v_max3_u32 v49, v49, v74, v75\n\t"
      "v_max3_u32 v49, v49, v76, v77\n\t"
      "v_max3_u32 v49, v49, v78, v79\n\t"
      "v_max3_u32 v49, v49, v80, v81\n\t"
      "v_max3_u32 v49, v49, v82, v83\n\t"
      "v_max3_u32 v49, v49, v84, v85\n\t"
      "v_max3_u32 v49, v49, v86, v87\n\t"
      "v_cmp_eq_u32 vcc, -1, v49\n\t"
      "s_cbranch_vccnz .Ls2l_4_%=\n\t"
      "s_branch .Lr2_4_%=\n\t"
      ".Lf2_4_%=:\n\t"
      "s_mov_b32 %[fail], 0\n\t"
      "s_branch .Lr2_4_%=\n\t"
      ".Ls2_5_%=:\n\t"
      "s_mov_b32 s65, 0\n\t"
      "s_add_u32 s63, %[hb], 36864\n\t"
      ".Ls2l_5_%=:\n\t"
      "s_add_u32 s65, s65, 1\n\t"
      "s_cmp_gt_u32 s65, %[fail]\n\t"
      "s_cbranch_scc1 .Lf2_5_%=\n\t"
      "buffer_load_dwordx4 v[88:91], v43, %[rs], s63 offen offset:0 sc1\n\t"
      "buffer_load_dwordx4 v[92:95], v44, %[rs], s63 offen offset:0 sc1\n\t"
      "buffer_load_dwordx4 v[96:99], v43, %[rs], s63 offen offset:1024 sc1\n\t"
      "buffer_load_dwordx4 v[100:103], v44, %[rs], s63 offen offset:1024 sc1\n\t"
      "buffer_load_dwordx4 v[104:107], v43, %[rs], s63 offen offset:2048 sc1\n\t"
      "buffer_load_dwordx4 v[108:111], v44, %[rs], s63 offen offset:2048 sc1\n\t"
      "s_waitcnt vmcnt(0)\n\t"
      "v_max3_u32 v49, v91, v88, v89\n\t"
      "v_max_u32 v49, v49, v90\n\t"
      "v_max3_u32 v49, v49, v92, v93\n\t"
      "v_max3_u32 v49, v49, v94, v95\n\t"
      "v_max3_u32 v49, v49, v96, v97\n\t"
      "v_max3_u32 v49, v49, v98, v99\n\t"
      "v_max3_u32 v49, v49, v100, v101\n\t"
      "v_max3_u32 v49, v49, v102, v103\n\t"
      "v_max3_u32 v49, v49, v104, v105\n\t"
      "v_max3_u32 v49, v49, v106, v107\n\t"
      "v_max3_u32 v49, v49, v108, v109\n\t"
      "v_max3_u32 v49, v49, v110, v111\n\t"
      "v_cmp_eq_u32 vcc, -1, v49\n\t"
      "s_cbranch_vccnz .Ls2l_5_%=\n\t"
      "s_branch .Lr2_5_%=\n\t"
      ".Lf2_5_%=:\n\t"
      "s_mov_b32 %[fail], 0\n\t"
      "s_branch .Lr2_5_%=\n\t"
      ".Ls2_6_%=:\n\t"
      "s_mov_b32 s65, 0\n\t"
      "s_add_u32 s63, %[hb], 61440\n\t"
      ".Ls2l_6_%=:\n\t"
      "s_add_u32 s65, s65, 1\n\t"
      "s_cmp_gt_u32 s65, %[fail]\n\t"
      "s_cbranch_scc1 .Lf2_6_%=\n\t"
      "buffer_load_dwordx4 v[208:211], v43, %[rs], s63 offen offset:0 sc1\n\t"
      "buffer_load_dwordx4 v[212:215], v44, %[rs], s63 offen offset:0 sc1\n\t"
      "buffer_load_dwordx4 v[216:219], v43, %[rs], s63 offen offset:1024 sc1\n\t"
      "buffer_load_dwordx4 v[220:223], v44, %[rs], s63 offen offset:1024 sc1\n\t"
      "buffer_load_dwordx4 v[224:227], v43, %[rs], s63 offen offset:2048 sc1\n\t"
      "buffer_load_dwordx4 v[228:231], v44, %[rs], s63 offen offset:2048 sc1\n\t"
      "s_waitcnt vmcnt(0)\n\t"
      "v_max3_u32 v49, v211, v208, v209\n\t"
      "v_max_u32 v49, v49, v210\n\t"
      "v_max3_u32 v49, v49, v212, v213\n\t"
      "v_max3_u32 v49, v49, v214, v215\n\t"
      "v_max3_u32 v49, v49, v216, v217\n\t"
      "v_max3_u32 v49, v49, v218, v219\n\t"
      "v_max3_u32 v49, v49, v220, v221\n\t"
      "v_max3_u32 v49, v49, v222, v223\n\t"
      "v_max3_u32 v49, v49, v224, v225\n\t"
      "v_max3_u32 v49, v49, v226, v227\n\t"
      "v_max3_u32 v49, v49, v228, v229\n\t"
      "v_max3_u32 v49, v49, v230, v231\n\t"
      "v_cmp_eq_u32 vcc, -1, v49\n\t"
      "s_cbranch_vccnz .Ls2l_6_%=\n\t"
      "s_branch .Lr2_6_%=\n\t"
      ".Lf2_6_%=:\n\t"
      "s_mov_b32 %[fail], 0\n\t"
      "s_branch .Lr2_6_%=\n\t"
      ".Ls2_7_%=:\n\t"
      "s_mov_b32 s65, 0\n\t"
      "s_add_u32 s63, %[hb], 86016\n\t"
      ".Ls2l_7_%=:\n\t"
      "s_add_u32 s65, s65, 1\n\t"
      "s_cmp_gt_u32 s65, %[fail]\n\t"
      "s_cbranch_scc1 .Lf2_7_%=\n\t"
      "buffer_load_dwordx4 v[232:235], v43, %[rs], s63 offen offset:0 sc1\n\t"
      "buffer_load_dwordx4 v[236:239], v44, %[rs], s63 offen offset:0 sc1\n\t"
      "buffer_load_dwordx4 v[240:243], v43, %[rs], s63 offen offset:1024 sc1\n\t"
      "buffer_load_dwordx4 v[244:247], v44, %[rs], s63 offen offset:1024 sc1\n\t"
      "buffer_load_dwordx4 v[248:251], v43, %[rs], s63 offen offset:2048 sc1\n\t"
      "buffer_load_dwordx4 v[252:255], v44, %[rs], s63 offen offset:2048 sc1\n\t"
      "s_waitcnt vmcnt(0)\n\t"
      "v_max3_u32 v49, v235, v232, v233\n\t"
      "v_max_u32 v49, v49, v234\n\t"
      "v_max3_u32 v49, v49, v236, v237\n\t"
      "v_max3_u32 v49, v49, v238, v239\n\t"
      "v_max3_u32 v49, v49, v240, v241\n\t"
      "v_max3_u32 v49, v49, v242, v243\n\t"
      "v_max3_u32 v49, v49, v244, v245\n\t"
      "v_max3_u32 v49, v49, v246, v247\n\t"
      "v_max3_u32 v49, v49, v248, v249\n\t"
      "v_max3_u32 v49, v49, v250, v251\n\t"
      "v_max3_u32 v49, v49, v252, v253\n\t"
      "v_max3_u32 v49, v49, v254, v255\n\t"
      "v_cmp_eq_u32 vcc, -1, v49\n\t"
      "s_cbranch_vccnz .Ls2l_7_%=\n\t"
      "s_branch .Lr2_7_%=\n\t"
      ".Lf2_7_%=:\n\t"
      "s_mov_b32 %[fail], 0\n\t"
      "s_branch .Lr2_7_%=\n\t"
      ".Ls1_g0_%=:\n\t"
      "s_mov_b32 s67, m0\n\t"
      "s_mov_b32 s65, 0\n\t"
      ".Ls1l_g0_%=:\n\t"
      "s_add_u32 s65, s65, 1\n\t"
      "s_cmp_gt_u32 s65, %[fail]\n\t"
      "s_cbranch_scc1 .Lf1_g0_%=\n\t"
      "s_add_u32 m0, s64, 0\n\t"
      "s_nop 0\n\t"
      "buffer_load_dwordx4 v42, %[rs], m0 offen sc1 lds\n\t"
      "buffer_load_dwordx4 v42, %[rs], m0 offen offset:1024 sc1 lds\n\t"
      "s_add_u32 m0, s64, 16384\n\t"
      "s_nop 0\n\t"
      "buffer_load_dwordx4 v42, %[rs], m0 offen sc1 lds\n\t"
      "buffer_load_dwordx4 v42, %[rs], m0 offen offset:1024 sc1 lds\n\t"
      "s_add_u32 m0, s64, 32768\n\t"
      "s_nop 0\n\t"
      "buffer_load_dwordx4 v42, %[rs], m0 offen sc1 lds\n\t"
      "buffer_load_dwordx4 v42, %[rs], m0 offen offset:1024 sc1 lds\n\t"
      "s_add_u32 m0, s64, 49152\n\t"
      "s_nop 0\n\t"
      "buffer_load_dwordx4 v42, %[rs], m0 offen sc1 lds\n\t"
      "buffer_load_dwordx4 v42, %[rs], m0 offen offset:1024 sc1 lds\n\t"
      "s_waitcnt vmcnt(0)\n\t"
      "ds_read_b128 v[112:115], v39 offset:0\n\t"
      "ds_read_b128 v[116:119], v39 offset:1024\n\t"
      "ds_read_b128 v[120:123], v39 offset:16384\n\t"
      "ds_read_b128 v[124:127], v39 offset:17408\n\t"
      "ds_read_b128 v[128:131], v39 offset:32768\n\t"
      "ds_read_b128 v[132:135], v39 offset:33792\n\t"
      "ds_read_b128 v[136:139], v39 offset:49152\n\t"
      "ds_read_b128 v[140:143], v39 offset:50176\n\t"
      "s_waitcnt lgkmcnt(0)\n\t"
      "v_max3_u32 v49, v115, v112, v113\n\t"
      "v_max_u32 v49, v49, v114\n\t"
      "v_max3_u32 v49, v49, v116, v117\n\t"
      "v_max3_u32 v49, v49, v118, v119\n\t"
      "v_max3_u32 v49, v49, v120, v121\n\t"
      "v_max3_u32 v49, v49, v122, v123\n\t"
      "v_max3_u32 v49, v49, v124, v125\n\t"
      "v_max3_u32 v49, v49, v126, v127\n\t"
      "v_max3_u32 v49, v49, v128, v129\n\t"
      "v_max3_u32 v49, v49, v130, v131\n\t"
      "v_max3_u32 v49, v49, v132, v133\n\t"
      "v_max3_u32 v49, v49, v134, v135\n\t"
      "v_max3_u32 v49, v49, v136, v137\n\t"
      "v_max3_u32 v49, v49, v138, v139\n\t"
      "v_max3_u32 v49, v49, v140, v141\n\t"
      "v_max3_u32 v49, v49, v142, v143\n\t"
      "v_cmp_eq_u32 vcc, -1, v49\n\t"
      "s_cbranch_vccnz .Ls1l_g0_%=\n\t"
      "s_mov_b32 m0, s67\n\t"
      "s_branch .Lr1_g0_%=\n\t"
      ".Lf1_g0_%=:\n\t"
      "s_mov_b32 %[fail], 0\n\t"
      "s_mov_b32 m0, s67\n\t"
      "s_branch .Lr1_g0_%=\n\t"
      ".Ls2_0_%=:\n\t"
      "s_mov_b32 s65, 0\n\t"
      "s_add_u32 s63, %[hb], 0\n\t"
      ".Ls2l_0_%=:\n\t"
      "s_add_u32 s65, s65, 1\n\t"
      "s_cmp_gt_u32 s65, %[fail]\n\t"
      "s_cbranch_scc1 .Lf2_0_%=\n\t"
      "buffer_load_dwordx4 v[64:67], v43, %[rs], s63 offen offset:0 sc1\n\t"
      "buffer_load_dwordx4 v[68:71], v44, %[rs], s63 offen offset:0 sc1\n\t"
      "buffer_load_dwordx4 v[72:75], v43, %[rs], s63 offen offset:1024 sc1\n\t"
      "buffer_load_dwordx4 v[76:79], v44, %[rs], s63 offen offset:1024 sc1\n\t"
      "buffer_load_dwordx4 v[80:83], v43, %[rs], s63 offen offset:2048 sc1\n\t"
      "buffer_load_dwordx4 v[84:87], v44, %[rs], s63 offen offset:2048 sc1\n\t"
      "s_waitcnt vmcnt(0)\n\t"
      "v_max3_u32 v49, v67, v64, v65\n\t"
      "v_max_u32 v49, v49, v66\n\t"
      "v_max3_u32 v49, v49, v68, v69\n\t"
      "v_max3_u32 v49, v49, v70, v71\n\t"
      "v_max3_u32 v49, v49, v72, v73\n\t"
      "v_max3_u32 v49, v49, v74, v75\n\t"
      "v_max3_u32 v49, v49, v76, v77\n\t"
      "v_max3_u32 v49, v49, v78, v79\n\t"
      "v_max3_u32 v49, v49, v80, v81\n\t"
      "v_max3_u32 v49, v49, v82, v83\n\t"
      "v_max3_u32 v49, v49, v84, v85\n\t"
      "v_max3_u32 v49, v49, v86, v87\n\t"
      "v_cmp_eq_u32 vcc, -1, v49\n\t"
      "s_cbranch_vccnz .Ls2l_0_%=\n\t"
      "s_branch .Lr2_0_%=\n\t"
      ".Lf2_0_%=:\n\t"
      "s_mov_b32 %[fail], 0\n\t"
      "s_branch .Lr2_0_%=\n\t"
      ".Ldone_%=:\n\t"

      : [fail] "+s"(cap)
      : [rs] "s"(rS), [ro] "s"(rO), [w] "s"(w), [db] "s"(OFF_H2 + mt * 163840), [w1b] "s"(w1base), [w2b] "s"(OFF_WG2 + (ct * 4 + w) * 61440),
        [hb] "s"(OFF_H + (m2 * 16 + 4 * w) * 24576 + 2 * half * 3072), [hown] "s"(OFF_H + (mt * 16 + nt) * 24576),
        [x1o] "s"((pi + 512 * w) * 1280), [fown] "s"(fb + 16 + w * 4), [tag] "s"(tag),
        [wv] "v"(wv), [ad] "v"((l15 >> 3) * 1024 + (l15 & 7) * 128 + ((g ^ (l15 & 7)) * 16)), [ho] "v"(g * 3072 + 3 * w * 256 + l15 * 16),
        [xw1] "v"(lane < 16 ? wv + 1024 : wv + 256 * 1280 - 256), [flo] "v"(OFF_FLG + fl2 * 64 + 16), [flo2] "v"(OFF_FLG + fl2 * 64 + 32), [oo] "v"(row * 1280 + ct * 160 + g * 16),
        [gbo] "v"(OFF_MODG + 12800 + ct * 320 + g * 32), [x0] "v"(x1r[0]), [x1] "v"(x1r[1]), [x2] "v"(x1r[2])
      : "memory", "vcc", "scc", ACLOB, "a240","a241","a242","a243","a244","a245","a246","a247","a248","a249","a250","a251","a252","a253","a254","a255", "s60", "s61", "s62", "s63", "s64", "s65", "s66", "s67", "s68", "s69", "s70", "s71", "s72", "s73", "s74", "s75", "s76", "s77", "s78", "s79", "s80", "s81", "s82", "s83", "s84", "s85", "s86", "s87", "v32", "v33", "v34", "v35", "v36", "v37", "v38", "v39", "v40", "v41", "v42", "v43", "v44", "v45", "v46", "v47", "v48", "v49", "v50", "v51", "v52", "v53", "v54", "v55", "v56", "v57", "v58", "v59", "v60", "v61", "v62", "v63", "v64", "v65", "v66", "v67", "v68", "v69", "v70", "v71", "v72", "v73", "v74", "v75", "v76", "v77", "v78", "v79", "v80", "v81", "v82", "v83", "v84", "v85", "v86", "v87", "v88", "v89", "v90", "v91", "v92", "v93", "v94", "v95", "v96", "v97", "v98", "v99", "v100", "v101", "v102", "v103", "v104", "v105", "v106", "v107", "v108", "v109", "v110", "v111", "v112", "v113", "v114", "v115", "v116", "v117", "v118", "v119", "v120", "v121", "v122", "v123", "v124", "v125", "v126", "v127", "v128", "v129", "v130", "v131", "v132", "v133", "v134", "v135", "v136", "v137", "v138", "v139", "v140", "v141", "v142", "v143", "v144", "v145", "v146", "v147", "v148", "v149", "v150", "v151", "v152", "v153", "v154", "v155", "v156", "v157", "v158", "v159", "v160", "v161", "v162", "v163", "v164", "v165", "v166", "v167", "v168", "v169", "v170", "v171", "v172", "v173", "v174", "v175", "v176", "v177", "v178", "v179", "v180", "v181", "v182", "v183", "v184", "v185", "v186", "v187", "v188", "v189", "v190", "v191", "v192", "v193", "v194", "v195", "v196", "v197", "v198", "v199", "v200", "v201", "v202", "v203", "v204", "v205", "v206", "v207", "v208", "v209", "v210", "v211", "v212", "v213", "v214", "v215", "v216", "v217", "v218", "v219", "v220", "v221", "v222", "v223", "v224", "v225", "v226", "v227", "v228", "v229", "v230", "v231", "v232", "v233", "v234", "v235", "v236", "v237", "v238", "v239", "v240", "v241", "v242", "v243", "v244", "v245", "v246", "v247", "v248", "v249", "v250", "v251", "v252", "v253", "v254", "v255");
  }
  asm volatile(".Lk_end:");
}

extern "C" void kernel_launch(void* const* d_in, const int* in_sizes, int n_in, void* d_out, int out_size, void* d_ws, size_t ws_size, hipStream_t stream) {
  (void)hipFuncSetAttribute((const void*)fused_kernel, hipFuncAttributeMaxDynamicSharedMemorySize, LDS_BYTES);
  fused_kernel<<<256, 256, LDS_BYTES, stream>>>(d_in[0], d_in[1], d_in[2], d_in[4], d_in[5], d_in[6], d_in[7], d_in[8], d_out, d_ws);
}
```
